```python
import torch

def _softmax(x, axis=-1):
    return torch.softmax(x, dim=axis)
def _gelu(x):
    return torch.nn.functional.gelu(x, approximate="tanh")
def _silu(x):
    return torch.nn.functional.silu(x)
def _sigmoid(x):
    return torch.sigmoid(x)
def _layernorm(x, axis=-1, eps=1e-5):
    m = x.mean(dim=axis, keepdim=True)
    v = x.var(dim=axis, keepdim=True, unbiased=False)
    return (x - m) / torch.sqrt(v + eps)
def _scatter_add(tgt, idx, src, axis=0):
    return tgt.clone().index_add_(axis, idx, src)
def _segment_sum(src, seg, n):
    out = torch.zeros((n,) + src.shape[1:], dtype=src.dtype)
    return out.index_add_(0, seg, src)
def _pad(x, pw):
    flat = tuple(v for pair in reversed(pw) for v in pair)
    return torch.nn.functional.pad(x, flat)

kernel_name = 'concat_heads__mha_block_cached__gqa_block_cached__near_ridge'

def setup_inputs():

    _w = (lambda a: (lambda t: t.to(torch.bfloat16) if t.is_floating_point() else t)(torch.as_tensor(a)))
    def _A_setup():
        B, Sq, D = 8, 112, 512
        H, Sk, Dh = 32, 512, 128
        return {
            "x": _w((lambda *s: torch.randn(*s, dtype=torch.bfloat16) * 0.8)(B, Sq, D)),
            "k_cache": _w((lambda *s: torch.randn(*s, dtype=torch.bfloat16) * 0.8)(B, H, Sk, Dh)),
            "v_cache": _w((lambda *s: torch.randn(*s, dtype=torch.bfloat16) * 0.05)(B, H, Sk, Dh)),
            "wq": _w((lambda *s: torch.randn(*s, dtype=torch.bfloat16) * 0.8)(D, H * Dh) * (D ** -0.5)),
            "wo": _w((lambda *s: torch.randn(*s, dtype=torch.bfloat16) * 0.05)(H * Dh, D) * ((H * Dh) ** -0.5)),
        }
    _A_in = _A_setup()

    def _B_setup():
        B, Sq, D = 8, 112, 512
        Hq, Hkv, Sk, Dh = 32, 32, 512, 128
        assert Hq % Hkv == 0, (Hq, Hkv)
        return {
            "x": _w((lambda *s: torch.randn(*s, dtype=torch.bfloat16) * 0.8)(B, Sq, D)),
            "k_cache": _w((lambda *s: torch.randn(*s, dtype=torch.bfloat16) * 0.8)(B, Hkv, Sk, Dh)),
            "v_cache": _w((lambda *s: torch.randn(*s, dtype=torch.bfloat16) * 0.05)(B, Hkv, Sk, Dh)),
            "wq": _w((lambda *s: torch.randn(*s, dtype=torch.bfloat16) * 0.8)(D, Hq * Dh) * (D ** -0.5)),
            "wo": _w((lambda *s: torch.randn(*s, dtype=torch.bfloat16) * 0.05)(Hq * Dh, D) * ((Hq * Dh) ** -0.5)),
        }
    _B_in = _B_setup()

    return {
        "A_x": _A_in["x"],
        "A_k_cache": _A_in["k_cache"],
        "A_v_cache": _A_in["v_cache"],
        "A_wq": _A_in["wq"],
        "A_wo": _A_in["wo"],
        "B_x": _B_in["x"],
        "B_k_cache": _B_in["k_cache"],
        "B_v_cache": _B_in["v_cache"],
        "B_wq": _B_in["wq"],
        "B_wo": _B_in["wo"],
    }

def reference(A_x, A_k_cache, A_v_cache, A_wq, A_wo, B_x, B_k_cache, B_v_cache, B_wq, B_wo):
    def _A_ref(x, k_cache, v_cache, wq, wo):
        B, Sq, D = x.shape
        H, Dh = 32, 128
        q = (lambda x, *d: x.permute(*d))((x @ wq).reshape(B, Sq, H, Dh), 0, 2, 1, 3)
        scores = torch.einsum("bhqd,bhkd->bhqk", q, k_cache) * (Dh ** -0.5)
        p = _softmax(scores, axis=-1)
        o = torch.einsum("bhqk,bhkd->bhqd", p, v_cache)
        return (lambda x, *d: x.permute(*d))(o, 0, 2, 1, 3).reshape(B, Sq, H * Dh) @ wo

    def _B_ref(x, k_cache, v_cache, wq, wo):
        B, Sq, D = x.shape
        Hq, Dh = 32, 128
        Hkv = k_cache.shape[1]
        rep = Hq // Hkv
        q = (lambda x, *d: x.permute(*d))((x @ wq).reshape(B, Sq, Hq, Dh), 0, 2, 1, 3)
        k = (lambda x, n, axis: torch.repeat_interleave(x, n, dim=axis))(k_cache, rep, axis=1)
        v = (lambda x, n, axis: torch.repeat_interleave(x, n, dim=axis))(v_cache, rep, axis=1)
        scores = torch.einsum("bhqd,bhkd->bhqk", q, k) * (Dh ** -0.5)
        p = _softmax(scores, axis=-1)
        o = torch.einsum("bhqk,bhkd->bhqd", p, v)
        return (lambda x, *d: x.permute(*d))(o, 0, 2, 1, 3).reshape(B, Sq, Hq * Dh) @ wo

    a = _A_ref(A_x, A_k_cache, A_v_cache, A_wq, A_wo)
    b = _B_ref(B_x, B_k_cache, B_v_cache, B_wq, B_wo)
    return torch.concatenate([a, b], axis=-1)
```

```cpp
#include <hip/hip_runtime.h>
#include <hip/hip_bf16.h>

typedef __bf16 bf16x8 __attribute__((ext_vector_type(8)));
typedef __bf16 bf16x2 __attribute__((ext_vector_type(2)));
typedef short s16x4 __attribute__((ext_vector_type(4)));
typedef short s16x8 __attribute__((ext_vector_type(8)));
typedef float f32x16 __attribute__((ext_vector_type(16)));
typedef float f32x4 __attribute__((ext_vector_type(4)));
typedef float f32x2 __attribute__((ext_vector_type(2)));
typedef unsigned u32x4 __attribute__((ext_vector_type(4)));
typedef unsigned u32x2 __attribute__((ext_vector_type(2)));
typedef unsigned short u16;
#define LAS __attribute__((address_space(3)))
#define DI __device__ __forceinline__
#define MFMA(a, b, c) __builtin_amdgcn_mfma_f32_32x32x16_bf16((a), (b), (c), 0, 0, 0)

constexpr int NB = 8, SQ = 112, DM = 512, NH = 32, SK = 512, DH = 128, HD = NH * DH;
constexpr unsigned SLOT = 16384;
constexpr int LDS_BYTES = 163840;
constexpr float SM_C = 0.08838834764831845f * 1.4426950408889634f;

constexpr size_t WS_O = 0;
constexpr size_t WS_O_BYTES = (size_t)2 * NB * 4 * 256 * 1024;
constexpr size_t WS_P = WS_O + WS_O_BYTES;
constexpr size_t WS_P_BYTES = (size_t)4 * 2 * NB * 4 * 65536;

DI unsigned rfl(unsigned x) { return __builtin_amdgcn_readfirstlane(x); }
DI unsigned cvtpk(float lo, float hi) { f32x2 v = {lo, hi}; bf16x2 b = __builtin_convertvector(v, bf16x2); return __builtin_bit_cast(unsigned, b); }
DI bf16x8 pack8(const f32x16& x, int base) {
  u32x4 w = {cvtpk(x[base + 0], x[base + 1]), cvtpk(x[base + 2], x[base + 3]), cvtpk(x[base + 4], x[base + 5]), cvtpk(x[base + 6], x[base + 7])};
  return __builtin_bit_cast(bf16x8, w);
}
DI s16x4 trread(unsigned a) { return __builtin_amdgcn_ds_read_tr16_b64_v4i16((LAS s16x4*)(size_t)a); }
DI bf16x8 trfrag(unsigned a0, unsigned a1) {
  s16x4 lo = trread(a0), hi = trread(a1);
  s16x8 r = __builtin_shufflevector(lo, hi, 0, 1, 2, 3, 4, 5, 6, 7);
  return __builtin_bit_cast(bf16x8, r);
}
DI bf16x8 ldsfrag(unsigned a) { return *(const LAS bf16x8*)(size_t)a; }
DI float swap32_max(float x) { auto r = __builtin_amdgcn_permlane32_swap(__float_as_uint(x), __float_as_uint(x), false, false); return fmaxf(__uint_as_float(r[0]), __uint_as_float(r[1])); }
DI float swap32_add(float x) { auto r = __builtin_amdgcn_permlane32_swap(__float_as_uint(x), __float_as_uint(x), false, false); return __uint_as_float(r[0]) + __uint_as_float(r[1]); }

typedef __amdgpu_buffer_rsrc_t rsrc_t;
DI rsrc_t mkrsrc(const void* p, unsigned bytes) { return __builtin_amdgcn_make_buffer_rsrc((void*)p, (short)0, (int)bytes, 0x00020000); }

DI void dma4_lin(rsrc_t r, unsigned voff, unsigned soff, unsigned m0v) {
  unsigned keep;
  asm volatile("s_mov_b32 %0, m0\n\ts_mov_b32 m0, %4\n\ts_nop 2\n\t"
               "buffer_load_dwordx4 %1, %2, %3 offen lds\n\t"
               "buffer_load_dwordx4 %1, %2, %3 offen offset:1024 lds\n\t"
               "buffer_load_dwordx4 %1, %2, %3 offen offset:2048 lds\n\t"
               "buffer_load_dwordx4 %1, %2, %3 offen offset:3072 lds\n\t"
               "s_mov_b32 m0, %0"
               : "=&s"(keep) : "v"(voff), "s"(r), "s"(soff), "s"(m0v) : "memory");
}
DI void dma4_sep(rsrc_t r, unsigned v0, unsigned v1, unsigned v2, unsigned v3, unsigned soff, unsigned m0v) {
  unsigned keep;
  asm volatile("s_mov_b32 %0, m0\n\ts_mov_b32 m0, %7\n\ts_nop 2\n\t"
               "buffer_load_dwordx4 %1, %5, %6 offen lds\n\t"
               "buffer_load_dwordx4 %2, %5, %6 offen offset:1024 lds\n\t"
               "buffer_load_dwordx4 %3, %5, %6 offen offset:2048 lds\n\t"
               "buffer_load_dwordx4 %4, %5, %6 offen offset:3072 lds\n\t"
               "s_mov_b32 m0, %0"
               : "=&s"(keep) : "v"(v0), "v"(v1), "v"(v2), "v"(v3), "s"(r), "s"(soff), "s"(m0v) : "memory");
}
DI void dma2(rsrc_t r, unsigned v0, unsigned v1, unsigned s0, unsigned s1, unsigned m0v) {
  unsigned keep;
  asm volatile("s_mov_b32 %0, m0\n\ts_mov_b32 m0, %6\n\ts_nop 2\n\t"
               "buffer_load_dwordx4 %1, %3, %4 offen lds\n\t"
               "buffer_load_dwordx4 %2, %3, %5 offen offset:1024 lds\n\t"
               "s_mov_b32 m0, %0"
               : "=&s"(keep) : "v"(v0), "v"(v1), "s"(r), "s"(s0), "s"(s1), "s"(m0v) : "memory");
}
#define WAIT_BAR(N) asm volatile("s_waitcnt vmcnt(" #N ") lgkmcnt(0)\n\ts_barrier" ::: "memory")

#define KV_K0C -1
#define KV_V0C -1

__global__ __launch_bounds__(512, 2) void attn_kernel(const u16* __restrict__ xA, const u16* __restrict__ kA, const u16* __restrict__ vA, const u16* __restrict__ wqA,
                                                      const u16* __restrict__ xB, const u16* __restrict__ kB, const u16* __restrict__ vB, const u16* __restrict__ wqB,
                                                      u16* __restrict__ Of) {
  extern __shared__ __attribute__((aligned(16))) char lds[];
  const unsigned tid = threadIdx.x, lane = tid & 63, r32 = lane & 31, hi = lane >> 5;
  const unsigned w = rfl(tid >> 6), g = w >> 2, wq = w & 3;
  const unsigned xcd = blockIdx.x & 7, idx = blockIdx.x >> 3;
  const unsigned br = xcd >> 2, hp = (xcd & 3) * 4 + (idx & 3), b = idx >> 2;
  const unsigned head = 2 * hp + g;
  const unsigned ldsb = (unsigned)(size_t)lds;

  const u16* xp = (br ? xB : xA) + (size_t)b * SQ * DM;
  const u16* wp = (br ? wqB : wqA) + (size_t)hp * 256;
  const u16* kp = (br ? kB : kA) + ((size_t)b * NH + head) * SK * DH;
  const u16* vp = (br ? vB : vA) + ((size_t)b * NH + head) * SK * DH;
  const rsrc_t rX = mkrsrc(xp, SQ * DM * 2);
  const rsrc_t rW = mkrsrc(wp, (DM * HD - hp * 256) * 2);
  const rsrc_t rK = mkrsrc(kp, SK * DH * 2);
  const rsrc_t rV = mkrsrc(vp, SK * DH * 2);

  const unsigned l4 = lane >> 4, l15 = lane & 15;
  const unsigned kbase = l4 * 256 + 16 * (l15 ^ l4);
  const unsigned vK0 = kbase, vK1 = kbase ^ 64, vK2 = kbase ^ 128, vK3 = kbase ^ 192;
  const unsigned vV = l4 * 256 + 16 * (l15 ^ (l4 << 2));
  const unsigned vW = l4 * 8192 + 16 * (l15 ^ (l4 << 2));
  const unsigned vX0 = (16 * w + (lane >> 3)) * 1024 + 16 * ((lane & 7) ^ l4);
  const unsigned vX1 = (16 * w + 8 + (lane >> 3)) * 1024 + 16 * ((lane & 7) ^ (4 + l4)) - 1024;
  const unsigned ring0 = ldsb + g * 5 * SLOT;
  auto issueW = [&](unsigned u, unsigned buf) {
    const unsigned s0 = g * 256 + (32 * u + 8 * wq) * 8192;
    dma2(rW, vW, vW, s0, s0 + 4 * 8192 - 1024, buf + w * 2048);
  };
  auto issueX = [&](unsigned c, unsigned buf) {
    const unsigned s0 = c * 128;
    dma2(rX, vX0, vX1, s0, s0, buf + w * 2048);
  };
  auto issueK = [&](unsigned j, unsigned slot) { dma4_sep(rK, vK0, vK1, vK2, vK3, j * SLOT + wq * 4096, slot + wq * 4096); };
  auto issueV = [&](unsigned j, unsigned slot) { dma4_lin(rV, vV, j * SLOT + wq * 4096, slot + wq * 4096); };

  const unsigned tq = l15 >> 2, tp = lane & 3, blk = l4 & 1;
  unsigned trb[4];
#pragma unroll
  for (int c = 0; c < 4; ++c) trb[c] = 256 * tq + 64 * (c ^ tq) + 32 * blk + 16 * (tp & 1) + 8 * (tp >> 1);
  const unsigned xrow = 32 * wq + r32;
  const unsigned xa0 = 128 * xrow + 16 * (hi ^ ((xrow >> 1) & 7));
  const unsigned ka0 = 256 * r32 + 16 * (hi ^ (r32 & 15));

  const unsigned WB0 = ldsb + 2 * SLOT, WB1 = ldsb + 7 * SLOT, XB0 = ldsb + 4 * SLOT;
  issueW(0, WB0); issueX(0, XB0); issueW(1, WB1); issueW(2, WB0 + SLOT);
  if (KV_K0C < 0) issueK(0, ring0);
  if (KV_V0C < 0) issueV(0, ring0 + SLOT);
  f32x16 acc[4];
#pragma unroll
  for (int c = 0; c < 4; ++c) acc[c] = f32x16{};
  for (unsigned c = 0; c < 8; ++c) {
    const unsigned par = (c & 1) * SLOT;
    const unsigned xb = XB0 + 5 * par;
    if (c < 7) { WAIT_BAR(4); } else { WAIT_BAR(2); }
    if (c < 7) issueX(c + 1, XB0 + 5 * (SLOT - par));
    if (c < 7) issueW(2 * c + 3, WB1 + (SLOT - par));
    if (c == 7) issueK(1, ring0 + 2 * SLOT);
    {
      const unsigned wb = WB0 + par + g * 8192 + hi * 2048;
#pragma unroll
      for (int s = 0; s < 2; ++s) {
        const bf16x8 xf = ldsfrag(xb + (xa0 ^ (32 * s)));
#pragma unroll
        for (int d = 0; d < 4; ++d) acc[d] = MFMA(trfrag(wb + trb[d] + 4096 * s, wb + trb[d] + 4096 * s + 1024), xf, acc[d]);
      }
    }
    if (c < 7) { WAIT_BAR(6); } else { WAIT_BAR(4); }
    if (c < 6) issueW(2 * c + 4, WB0 + par);
    if ((int)c == KV_K0C) issueK(0, ring0);
    if ((int)c == KV_V0C) issueV(0, ring0 + SLOT);
    {
      const unsigned wb = WB1 + par + g * 8192 + hi * 2048;
#pragma unroll
      for (int s = 0; s < 2; ++s) {
        const bf16x8 xf = ldsfrag(xb + (xa0 ^ (32 * (2 + s))));
#pragma unroll
        for (int d = 0; d < 4; ++d) acc[d] = MFMA(trfrag(wb + trb[d] + 4096 * s, wb + trb[d] + 4096 * s + 1024), xf, acc[d]);
      }
    }
  }
  bf16x8 qf[8];
#pragma unroll
  for (int c = 0; c < 4; ++c) { qf[2 * c] = pack8(acc[c], 0); qf[2 * c + 1] = pack8(acc[c], 8); }

#pragma unroll
  for (int c = 0; c < 4; ++c) acc[c] = f32x16{};
  float m = -__builtin_inff(), l = 0.f;
  const unsigned ringend = ring0 + 5 * SLOT;
  unsigned cur = ring0, fre = ring0 + 4 * SLOT;
  for (unsigned j = 0; j < 8; ++j) {
    if (j < 7) { WAIT_BAR(12); } else { WAIT_BAR(4); }
    if (j == 0) issueV(1, ring0 + 3 * SLOT);
    if (j < 6) issueK(j + 2, fre);
    f32x16 s0 = f32x16{}, s1 = f32x16{};
#pragma unroll
    for (int s = 0; s < 8; ++s) {
      const unsigned a = cur + (ka0 ^ (32 * s));
      s0 = MFMA(ldsfrag(a), qf[s], s0);
      s1 = MFMA(ldsfrag(a + 8192), qf[s], s1);
    }
    fre = cur; cur += SLOT; if (cur == ringend) cur = ring0;
    float mx = fmaxf(s0[0], s1[0]);
#pragma unroll
    for (int r = 1; r < 16; ++r) mx = fmaxf(mx, fmaxf(s0[r], s1[r]));
    mx = swap32_max(mx);
    const float mn = fmaxf(m, mx);
    const float alpha = __builtin_amdgcn_exp2f((m - mn) * SM_C);
    const float nm = -mn * SM_C;
    m = mn;
    float ps = 0.f;
#pragma unroll
    for (int r = 0; r < 16; ++r) { s0[r] = __builtin_amdgcn_exp2f(fmaf(s0[r], SM_C, nm)); ps += s0[r]; }
#pragma unroll
    for (int r = 0; r < 16; ++r) { s1[r] = __builtin_amdgcn_exp2f(fmaf(s1[r], SM_C, nm)); ps += s1[r]; }
    l = l * alpha + ps;
    bf16x8 pf[4];
    pf[0] = pack8(s0, 0); pf[1] = pack8(s0, 8); pf[2] = pack8(s1, 0); pf[3] = pack8(s1, 8);
#pragma unroll
    for (int c = 0; c < 4; ++c) acc[c] *= alpha;
    if (j < 6) { WAIT_BAR(12); } else if (j == 6) { WAIT_BAR(8); } else { WAIT_BAR(0); }
    if (j < 6) issueV(j + 2, fre);
    {
      const unsigned vb = cur + hi * 1024;
#pragma unroll
      for (int ks = 0; ks < 4; ++ks)
#pragma unroll
        for (int c = 0; c < 4; ++c) acc[c] = MFMA(trfrag(vb + trb[c] + 4096 * ks, vb + trb[c] + 4096 * ks + 2048), pf[ks], acc[c]);
    }
    fre = cur; cur += SLOT; if (cur == ringend) cur = ring0;
  }
  const float inv = 1.f / swap32_add(l);
  u16* op = Of + (((((size_t)br * NB + b) * 4 + wq) * 256 + head * 8) * 64 + lane) * 8;
#pragma unroll
  for (int c = 0; c < 4; ++c) {
    acc[c] *= inv;
    *(bf16x8*)(op + (2 * c) * 512) = pack8(acc[c], 0);
    *(bf16x8*)(op + (2 * c + 1) * 512) = pack8(acc[c], 8);
  }
}

__global__ __launch_bounds__(512, 2) void oproj_kernel(const u16* __restrict__ Of, const u16* __restrict__ woA, const u16* __restrict__ woB, float* __restrict__ P) {
  extern __shared__ __attribute__((aligned(16))) char lds[];
  const unsigned tid = threadIdx.x, lane = tid & 63, hi = lane >> 5;
  const unsigned w = rfl(tid >> 6), g = w >> 2, wq = w & 3;
  const unsigned xcd = blockIdx.x & 7, idx = blockIdx.x >> 3;
  const unsigned br = xcd >> 2, kq = xcd & 3, nb = idx & 3, b = idx >> 2;
  const unsigned ldsb = (unsigned)(size_t)lds;
  const u16* wp = (br ? woB : woA) + (size_t)(kq * 1024) * DM + nb * 128;
  const rsrc_t rW = mkrsrc(wp, (1024 * DM - nb * 128) * 2);
  const unsigned l4 = lane >> 4, l15 = lane & 15;
  const unsigned vW = l4 * 1024 + 16 * (l15 ^ (l4 << 2));
  auto issueW = [&](unsigned u, unsigned buf) {
    const unsigned s0 = (512 * g + 32 * u + 8 * wq) * 1024;
    dma2(rW, vW, vW, s0, s0 + 4 * 1024 - 1024, buf + w * 2048);
  };
  const unsigned tq = l15 >> 2, tp = lane & 3, blk = l4 & 1;
  unsigned trb[4];
#pragma unroll
  for (int c = 0; c < 4; ++c) trb[c] = 256 * tq + 64 * (c ^ tq) + 32 * (tp & 1) + 16 * blk + 8 * (tp >> 1);
  issueW(0, ldsb); issueW(1, ldsb + SLOT);
  const u16* op = Of + (((((size_t)br * NB + b) * 4 + wq) * 256 + kq * 64 + g * 32) * 64 + lane) * 8;
  bf16x8 of[32];
#pragma unroll
  for (int s = 0; s < 32; ++s) of[s] = *(const bf16x8*)(op + s * 512);
#pragma unroll
  for (int u = 2; u < 10; ++u) issueW(u, ldsb + u * SLOT);
  f32x16 acc[4];
#pragma unroll
  for (int c = 0; c < 4; ++c) acc[c] = f32x16{};
#pragma unroll
  for (int u = 0; u < 16; ++u) {
    if (u <= 7) { WAIT_BAR(16); } else if (u == 8) { WAIT_BAR(14); } else if (u == 9) { WAIT_BAR(12); } else if (u == 10) { WAIT_BAR(10); } else if (u == 11) { WAIT_BAR(8); }
    else if (u == 12) { WAIT_BAR(6); } else if (u == 13) { WAIT_BAR(4); } else if (u == 14) { WAIT_BAR(2); } else { WAIT_BAR(0); }
    if (u >= 1 && u + 9 < 16) issueW(u + 9, ldsb + ((u + 9) % 10) * SLOT);
    const unsigned wb = ldsb + (u % 10) * SLOT + g * 8192 + hi * 2048;
#pragma unroll
    for (int s = 0; s < 2; ++s)
#pragma unroll
      for (int d = 0; d < 4; ++d) acc[d] = MFMA(trfrag(wb + trb[d] + 4096 * s, wb + trb[d] + 4096 * s + 1024), of[2 * u + s], acc[d]);
  }
  asm volatile("s_waitcnt lgkmcnt(0)\n\ts_barrier" ::: "memory");
  {
    LAS f32x4* xb = (LAS f32x4*)(size_t)(ldsb + w * 8192 + lane * 16);
#pragma unroll
    for (int c = 0; c < 2; ++c)
#pragma unroll
      for (int q4 = 0; q4 < 4; ++q4) {
        const f32x16& a = g ? acc[c] : acc[2 + c];
        xb[(c * 4 + q4) * 64] = f32x4{a[4 * q4], a[4 * q4 + 1], a[4 * q4 + 2], a[4 * q4 + 3]};
      }
  }
  asm volatile("s_waitcnt lgkmcnt(0)\n\ts_barrier" ::: "memory");
  {
    const LAS f32x4* xb = (const LAS f32x4*)(size_t)(ldsb + (w ^ 4) * 8192 + lane * 16);
    float* pp = P + ((((((size_t)kq * 2 + br) * NB + b) * 4 + nb) * 4 + wq) * 4 + 2 * g) * 1024 + lane * 4;
#pragma unroll
    for (int c = 0; c < 2; ++c)
#pragma unroll
      for (int q4 = 0; q4 < 4; ++q4) {
        const f32x16& a = g ? acc[2 + c] : acc[c];
        const f32x4 o = xb[(c * 4 + q4) * 64];
        *(f32x4*)(pp + (c * 4 + q4) * 256) = f32x4{a[4 * q4] + o[0], a[4 * q4 + 1] + o[1], a[4 * q4 + 2] + o[2], a[4 * q4 + 3] + o[3]};
      }
  }
}

__global__ __launch_bounds__(256) void combine_kernel(const float* __restrict__ P, u16* __restrict__ out) {
  const unsigned t = blockIdx.x * 4 + (threadIdx.x >> 6), lane = threadIdx.x & 63;
  const unsigned c = t & 3, wq = (t >> 2) & 3, nb = (t >> 4) & 3, b = (t >> 6) & 7, br = t >> 9;
  const unsigned q = 32 * wq + (lane & 31), hi = lane >> 5;
  if (q >= SQ) return;
  const float* pp = P + (size_t)t * 1024 + lane * 4;
  f32x4 s[4];
#pragma unroll
  for (int q4 = 0; q4 < 4; ++q4) {
    f32x4 a = *(const f32x4*)(pp + q4 * 256);
#pragma unroll
    for (int k = 1; k < 4; ++k) a += *(const f32x4*)(pp + (size_t)k * 1024 * 1024 + q4 * 256);
    s[q4] = a;
  }
  u16* o = out + ((size_t)b * SQ + q) * 1024 + br * 512 + nb * 128 + c * 32 + hi * 16;
  *(u32x4*)o = u32x4{cvtpk(s[0][0], s[0][1]), cvtpk(s[0][2], s[0][3]), cvtpk(s[1][0], s[1][1]), cvtpk(s[1][2], s[1][3])};
  *(u32x4*)(o + 8) = u32x4{cvtpk(s[2][0], s[2][1]), cvtpk(s[2][2], s[2][3]), cvtpk(s[3][0], s[3][1]), cvtpk(s[3][2], s[3][3])};
}

extern "C" void kernel_launch(void* const* d_in, const int* in_sizes, int n_in, void* d_out, int out_size, void* d_ws, size_t ws_size, hipStream_t stream) {
  const u16* xA = (const u16*)d_in[0]; const u16* kA = (const u16*)d_in[1]; const u16* vA = (const u16*)d_in[2]; const u16* wqA = (const u16*)d_in[3]; const u16* woA = (const u16*)d_in[4];
  const u16* xB = (const u16*)d_in[5]; const u16* kB = (const u16*)d_in[6]; const u16* vB = (const u16*)d_in[7]; const u16* wqB = (const u16*)d_in[8]; const u16* woB = (const u16*)d_in[9];
  u16* Of = (u16*)((char*)d_ws + WS_O);
  float* P = (float*)((char*)d_ws + WS_P);
  (void)hipFuncSetAttribute((const void*)attn_kernel, hipFuncAttributeMaxDynamicSharedMemorySize, LDS_BYTES);
  (void)hipFuncSetAttribute((const void*)oproj_kernel, hipFuncAttributeMaxDynamicSharedMemorySize, LDS_BYTES);
  attn_kernel<<<256, 512, LDS_BYTES, stream>>>(xA, kA, vA, wqA, xB, kB, vB, wqB, Of);
  oproj_kernel<<<256, 512, LDS_BYTES, stream>>>(Of, woA, woB, P);
  combine_kernel<<<256, 256, 0, stream>>>(P, (u16*)d_out);
}
```

```cpp
#include <hip/hip_runtime.h>
#include <hip/hip_bf16.h>

typedef __bf16 bf16x8 __attribute__((ext_vector_type(8)));
typedef __bf16 bf16x2 __attribute__((ext_vector_type(2)));
typedef short s16x4 __attribute__((ext_vector_type(4)));
typedef short s16x8 __attribute__((ext_vector_type(8)));
typedef float f32x16 __attribute__((ext_vector_type(16)));
typedef float f32x4 __attribute__((ext_vector_type(4)));
typedef float f32x2 __attribute__((ext_vector_type(2)));
typedef unsigned u32x4 __attribute__((ext_vector_type(4)));
typedef unsigned u32x2 __attribute__((ext_vector_type(2)));
typedef unsigned short u16;
#define LAS __attribute__((address_space(3)))
#define DI __device__ __forceinline__
#define MFMA(a, b, c) __builtin_amdgcn_mfma_f32_32x32x16_bf16((a), (b), (c), 0, 0, 0)

constexpr int NB = 8, SQ = 112, DM = 512, NH = 32, SK = 512, DH = 128, HD = NH * DH;
constexpr unsigned SLOT = 16384;
constexpr int LDS_BYTES = 163840;
constexpr float SM_C = 0.08838834764831845f * 1.4426950408889634f;
constexpr float SM_THR = 8.f;

constexpr size_t WS_O = 0;
constexpr size_t WS_O_BYTES = (size_t)2 * NB * 4 * 256 * 1024;
constexpr size_t WS_P = WS_O + WS_O_BYTES;
constexpr size_t WS_P_BYTES = (size_t)4 * 2 * NB * 4 * 65536;

DI unsigned rfl(unsigned x) { return __builtin_amdgcn_readfirstlane(x); }
DI unsigned cvtpk(float lo, float hi) { f32x2 v = {lo, hi}; bf16x2 b = __builtin_convertvector(v, bf16x2); return __builtin_bit_cast(unsigned, b); }
DI bf16x8 pack8(const f32x16& x, int base) {
  u32x4 w = {cvtpk(x[base + 0], x[base + 1]), cvtpk(x[base + 2], x[base + 3]), cvtpk(x[base + 4], x[base + 5]), cvtpk(x[base + 6], x[base + 7])};
  return __builtin_bit_cast(bf16x8, w);
}
DI s16x4 trread(unsigned a) { return __builtin_amdgcn_ds_read_tr16_b64_v4i16((LAS s16x4*)(size_t)a); }
DI bf16x8 cat(s16x4 lo, s16x4 hi) { s16x8 r = __builtin_shufflevector(lo, hi, 0, 1, 2, 3, 4, 5, 6, 7); return __builtin_bit_cast(bf16x8, r); }
DI bf16x8 ldsfrag(unsigned a) { return *(const LAS bf16x8*)(size_t)a; }
DI float swap32_add(float x) { auto r = __builtin_amdgcn_permlane32_swap(__float_as_uint(x), __float_as_uint(x), false, false); return __uint_as_float(r[0]) + __uint_as_float(r[1]); }
DI float max3f(float a, float b, float c) { return __builtin_fmaxf(__builtin_fmaxf(a, b), c); }

typedef __amdgpu_buffer_rsrc_t rsrc_t;
DI rsrc_t mkrsrc(const void* p, unsigned bytes) { return __builtin_amdgcn_make_buffer_rsrc((void*)p, (short)0, (int)bytes, 0x00020000); }

#define KVPOL "nt"
DI void dma4(rsrc_t r, unsigned voff, unsigned s0, unsigned s1, unsigned s2, unsigned s3, unsigned m0v) {
  unsigned keep;
  asm volatile("s_mov_b32 %0, m0\n\ts_mov_b32 m0, %7\n\ts_nop 0\n\t"
               "buffer_load_dwordx4 %1, %2, %3 offen lds\n\t"
               "buffer_load_dwordx4 %1, %2, %4 offen offset:1024 lds\n\t"
               "buffer_load_dwordx4 %1, %2, %5 offen offset:2048 lds\n\t"
               "buffer_load_dwordx4 %1, %2, %6 offen offset:3072 lds\n\t"
               "s_mov_b32 m0, %0"
               : "=&s"(keep) : "v"(voff), "s"(r), "s"(s0), "s"(s1), "s"(s2), "s"(s3), "s"(m0v) : "memory");
}
DI void dma3(rsrc_t r, unsigned voff, unsigned s0, unsigned s1, unsigned s2, unsigned m0v) {
  unsigned keep;
  asm volatile("s_mov_b32 %0, m0\n\ts_mov_b32 m0, %6\n\ts_nop 0\n\t"
               "buffer_load_dwordx4 %1, %2, %3 offen lds\n\t"
               "buffer_load_dwordx4 %1, %2, %4 offen offset:1024 lds\n\t"
               "buffer_load_dwordx4 %1, %2, %5 offen offset:2048 lds\n\t"
               "s_mov_b32 m0, %0"
               : "=&s"(keep) : "v"(voff), "s"(r), "s"(s0), "s"(s1), "s"(s2), "s"(m0v) : "memory");
}
DI void dma4_kv(rsrc_t r, unsigned v0, unsigned v1, unsigned v2, unsigned v3, unsigned soff, unsigned m0v) {
  unsigned keep;
  asm volatile("s_mov_b32 %0, m0\n\ts_mov_b32 m0, %7\n\ts_nop 0\n\t"
               "buffer_load_dwordx4 %1, %5, %6 offen " KVPOL " lds\n\t"
               "buffer_load_dwordx4 %2, %5, %6 offen offset:1024 " KVPOL " lds\n\t"
               "buffer_load_dwordx4 %3, %5, %6 offen offset:2048 " KVPOL " lds\n\t"
               "buffer_load_dwordx4 %4, %5, %6 offen offset:3072 " KVPOL " lds\n\t"
               "s_mov_b32 m0, %0"
               : "=&s"(keep) : "v"(v0), "v"(v1), "v"(v2), "v"(v3), "s"(r), "s"(soff), "s"(m0v) : "memory");
}
template <int N> DI void wait_bar() { asm volatile("s_waitcnt vmcnt(%0) lgkmcnt(0)\n\ts_barrier" :: "i"(N) : "memory"); }
template <int N> DI void wait_vm() { asm volatile("s_waitcnt vmcnt(%0)" :: "i"(N) : "memory"); }
#define SBAR() __builtin_amdgcn_sched_barrier(0)
#define BAR() asm volatile("s_barrier" ::: "memory")
#define LBAR() asm volatile("s_waitcnt lgkmcnt(0)\n\ts_barrier" ::: "memory")
#define PIN(x) asm volatile("" : "+v"(x))

#define STAMP_DECL
#define STAMP(i)
#define STAMPRT(i)
#define STAMP_OUT(k)
#define ROTQ 1
#define ROTK 1
#define KV_AT 0

struct Frags { s16x4 lo[8], hi[8]; bf16x8 b[2]; };
constexpr unsigned QBUF = 24576, QST = 2 * SLOT;
constexpr int NTHREADS = 640;

#define GRD_LO(F, i) F.lo[i] = trread((wb_ ^ (64 * ((i) & 3))) + 4096 * ((i) >> 2))
#define GRD_HI(F, i) F.hi[i] = trread((wb_ ^ (64 * ((i) & 3))) + 4096 * ((i) >> 2) + 1024)
#define GMM(F, i) acc[(i) & 3] = MFMA(cat(F.lo[i], F.hi[i]), F.b[(i) >> 2], acc[(i) & 3])
#define GREAD(FN, XR0, XR1) do { FN.b[0] = ldsfrag(XR0); FN.b[1] = ldsfrag(XR1); _Pragma("unroll") for (int i_ = 0; i_ < 8; ++i_) { GRD_LO(FN, i_); GRD_HI(FN, i_); } } while (0)
#define GSTEP(FC, FN, XR0, XR1) do { SBAR(); \
    GMM(FC, 0); FN.b[0] = ldsfrag(XR0); GRD_LO(FN, 0); GRD_HI(FN, 0); SBAR(); \
    GMM(FC, 1); GRD_LO(FN, 1); GRD_HI(FN, 1); GRD_LO(FN, 2); SBAR(); \
    GMM(FC, 2); GRD_HI(FN, 2); GRD_LO(FN, 3); GRD_HI(FN, 3); SBAR(); \
    GMM(FC, 3); FN.b[1] = ldsfrag(XR1); GRD_LO(FN, 4); GRD_HI(FN, 4); SBAR(); \
    GMM(FC, 4); GRD_LO(FN, 5); GRD_HI(FN, 5); GRD_LO(FN, 6); SBAR(); \
    GMM(FC, 5); GRD_HI(FN, 6); GRD_LO(FN, 7); GRD_HI(FN, 7); SBAR(); \
    GMM(FC, 6); SBAR(); \
    GMM(FC, 7); SBAR(); } while (0)

__global__ __launch_bounds__(NTHREADS, 3) void attn_kernel(const u16* __restrict__ xA, const u16* __restrict__ kA, const u16* __restrict__ vA, const u16* __restrict__ wqA,
                                                           const u16* __restrict__ xB, const u16* __restrict__ kB, const u16* __restrict__ vB, const u16* __restrict__ wqB,
                                                           u16* __restrict__ Of) {
  extern __shared__ __attribute__((aligned(16))) char lds[];
  const unsigned tid = threadIdx.x, lane = tid & 63;
  const unsigned w = rfl(tid >> 6);
  const unsigned xcd = blockIdx.x & 7, idx = blockIdx.x >> 3;
  const unsigned br = xcd >> 2, hp = (xcd & 3) * 4 + (idx & 3), b = idx >> 2;
  const unsigned ldsb = (unsigned)(size_t)lds;
  const unsigned l4 = lane >> 4, l15 = lane & 15;

  if (w >= 8) {
    const unsigned g = w - 8;
    const unsigned rotq = ROTQ ? 2 * b + 4 * (idx & 3) : 0;
    const unsigned kbase = l4 * 256 + 16 * (l15 ^ l4);
    const unsigned vK0 = kbase, vK1 = kbase ^ 64, vK2 = kbase ^ 128, vK3 = kbase ^ 192;
    const unsigned vV = l4 * 256 + 16 * (l15 ^ (l4 << 2));
    const size_t kvoff = ((size_t)b * NH + 2 * hp) * SK * DH;
    const rsrc_t rK = mkrsrc((br ? kB : kA) + kvoff, 2 * SK * DH * 2);
    const rsrc_t rV = mkrsrc((br ? vB : vA) + kvoff, 2 * SK * DH * 2);
    auto issueK = [&](unsigned gg, unsigned j, unsigned slot) {
      const unsigned so = gg * 8 * SLOT + ((j + (ROTK ? b + 2 * hp + gg : 0)) & 7) * SLOT;
#pragma unroll
      for (int i = 0; i < 4; ++i) dma4_kv(rK, vK0, vK1, vK2, vK3, so + i * 4096, slot + i * 4096);
    };
    auto issueV = [&](unsigned gg, unsigned j, unsigned slot) {
      const unsigned so = gg * 8 * SLOT + ((j + (ROTK ? b + 2 * hp + gg : 0)) & 7) * SLOT;
#pragma unroll
      for (int i = 0; i < 4; ++i) dma4_kv(rV, vV, vV, vV, vV, so + i * 4096, slot + i * 4096);
    };
    if (g == 0) {
      const rsrc_t rW = mkrsrc((br ? wqB : wqA) + (size_t)hp * 256, (DM * HD - hp * 256) * 2);
      const unsigned vW = l4 * 8192 + 16 * (l15 ^ (l4 << 2));
      auto issueW = [&](unsigned u) {
        const unsigned uu = (u + rotq) & 15, buf = ldsb + QST + (u & 3) * QBUF;
#pragma unroll
        for (int i = 0; i < 4; ++i) { const unsigned s0 = (i >> 1) * 256 + (32 * uu + 16 * (i & 1)) * 8192;
          dma4(rW, vW, s0, s0 + 32768 - 1024, s0 + 2 * 32768 - 2048, s0 + 3 * 32768 - 3072, buf + i * 4096); }
      };
      issueW(0); issueW(1); issueW(2);
      wait_vm<47>(); issueW(3);
      wait_bar<47>();
      for (unsigned u = 0; u < 12; ++u) { wait_bar<32>(); issueW(u + 4); }
      wait_bar<32>(); wait_bar<16>(); wait_bar<0>(); BAR();
    } else {
      const rsrc_t rX = mkrsrc((br ? xB : xA) + (size_t)b * SQ * DM, SQ * DM * 2);
      const unsigned vX = (lane >> 2) * 1024 + 16 * ((lane & 3) ^ l4);
      const unsigned vX2 = vX + 65536;
      auto issueX = [&](unsigned u) {
        const unsigned uu = (u + rotq) & 15, buf = ldsb + QST + (u & 3) * QBUF + 16384, s0 = uu * 64;
        dma4(rX, vX, s0, s0 + 16384 - 1024, s0 + 2 * 16384 - 2048, s0 + 3 * 16384 - 3072, buf);
        dma3(rX, vX2, s0, s0 + 16384 - 1024, s0 + 2 * 16384 - 2048, buf + 4096);
      };
      issueX(0); issueX(1); issueX(2); issueX(3);
      wait_bar<21>();
      auto kvhalf = [&](int t) {
        const unsigned gg = (t >> 1) & 1, so = gg * 8 * SLOT + ((ROTK ? b + 2 * hp + gg : 0) & 7) * SLOT + (t & 1) * 8192, sl = ldsb + gg * 8 * SLOT + (t >> 2) * SLOT + (t & 1) * 8192;
        if (t < 4) { dma4_kv(rK, vK0, vK1, vK2, vK3, so, sl); dma4_kv(rK, vK0, vK1, vK2, vK3, so + 4096, sl + 4096); }
        else { dma4_kv(rV, vV, vV, vV, vV, so, sl); dma4_kv(rV, vV, vV, vV, vV, so + 4096, sl + 4096); }
      };
#define KVN(s_) (((s_) >= KV_AT && (s_) < KV_AT + 8) ? 8 : 0)
#define XW(u) (14 + KVN((u) - 1) + KVN((u) - 2))
#define XSTEP(u) do { wait_bar<XW(u)>(); if (KVN(u)) kvhalf((u) - KV_AT); issueX((u) + 4); } while (0)
      static_assert(KV_AT >= 0 && KV_AT <= 2, "the K0 / V0 requests end by step 9");
      XSTEP(0); XSTEP(1); XSTEP(2); XSTEP(3); XSTEP(4); XSTEP(5); XSTEP(6); XSTEP(7); XSTEP(8); XSTEP(9); XSTEP(10); XSTEP(11);
      wait_bar<14>(); wait_bar<7>(); wait_bar<0>(); BAR();
    }
    const unsigned ringlo = ldsb + g * 5 * SLOT, ringhi = ringlo + 5 * SLOT, ring2 = ldsb + (2 + 3 * g) * SLOT;
    issueK(g, 1, ring2); issueV(g, 1, ring2 + SLOT); issueK(g, 2, ring2 + 2 * SLOT);
    if (g) BAR();
    unsigned fre = ldsb + g * 8 * SLOT;
    BAR();
    for (unsigned j = 0; j < 6; ++j) {
      wait_bar<47>(); issueV(g, j + 2, fre);
      fre += SLOT; if (fre == ringhi) fre = ringlo;
      wait_bar<47>(); if (j < 5) issueK(g, j + 3, fre);
      fre += SLOT; if (fre == ringhi) fre = ringlo;
    }
    wait_bar<32>(); wait_bar<16>(); wait_bar<0>();
    if (!g) BAR();
    return;
  }

  const unsigned r32 = lane & 31, hi = lane >> 5, g = w >> 2, wq = w & 3, head = 2 * hp + g;
  STAMP_DECL; STAMPRT(62); STAMP(0);
  const unsigned ringlo = ldsb + g * 5 * SLOT, ringhi = ringlo + 5 * SLOT, ring0 = ldsb + g * 8 * SLOT;
  const unsigned tq = l15 >> 2, tp = lane & 3, blk = l4 & 1;
  const unsigned tr0 = 256 * tq + 64 * tq + 32 * blk + 16 * (tp & 1) + 8 * (tp >> 1);
  const unsigned wq0 = g * 8192 + hi * 2048 + tr0;
  const unsigned xrow = (32 * wq + r32 < SQ) ? 32 * wq + r32 : 32 * wq + r32 - 16;
  const unsigned xa0 = 16384 + 64 * xrow + 16 * (hi ^ ((xrow >> 2) & 3));
  const unsigned ka0 = 256 * r32 + 16 * (hi ^ (r32 & 15));

  f32x16 acc[4];
#pragma unroll
  for (int c = 0; c < 4; ++c) acc[c] = f32x16{};
  Frags F0, F1;
  BAR();
  STAMP(1);
  { const unsigned buf_ = ldsb + QST, wb_ = buf_ + wq0; GREAD(F0, buf_ + xa0, (buf_ + xa0) ^ 32); }
  for (unsigned u = 0; u < 14; u += 2) {
    LBAR();
    { const unsigned buf_ = ldsb + QST + ((u + 1) & 3) * QBUF, wb_ = buf_ + wq0; GSTEP(F0, F1, buf_ + xa0, (buf_ + xa0) ^ 32); }
    LBAR();
    { const unsigned buf_ = ldsb + QST + ((u + 2) & 3) * QBUF, wb_ = buf_ + wq0; GSTEP(F1, F0, buf_ + xa0, (buf_ + xa0) ^ 32); }
    STAMP(2 + (u >> 1));
  }
  LBAR();
  { const unsigned buf_ = ldsb + QST + 3 * QBUF, wb_ = buf_ + wq0; GSTEP(F0, F1, buf_ + xa0, (buf_ + xa0) ^ 32); }
  LBAR();
  SBAR();
#pragma unroll
  for (int i = 0; i < 8; ++i) GMM(F1, i);
  bf16x8 qf[8];
#pragma unroll
  for (int c = 0; c < 4; ++c) { qf[2 * c] = pack8(acc[c], 0); qf[2 * c + 1] = pack8(acc[c], 8); }

#pragma unroll
  for (int c = 0; c < 4; ++c) acc[c] = f32x16{};
  float m = -__builtin_inff(), l = 0.f;
  unsigned cur = ring0;
  STAMP(9);
  if (g) BAR();
#define EXP2(v) __builtin_amdgcn_exp2f(__builtin_fmaf((v), SM_C, nm))
  for (unsigned j = 0; j < 8; ++j) {
    STAMP(10 + 4 * j);
    LBAR();
    STAMP(11 + 4 * j);
    SBAR(); __builtin_amdgcn_s_setprio(1);
    f32x16 s0 = f32x16{}, s1 = f32x16{}; bf16x8 kf[16]; const unsigned kb = cur + ka0;
#pragma unroll
    for (int i = 0; i < 4; ++i) kf[i] = ldsfrag((kb ^ (32 * (i >> 1))) + 8192 * (i & 1));
    SBAR();
#pragma unroll
    for (int i = 0; i < 16; ++i) {
      if (i & 1) s1 = MFMA(kf[i], qf[i >> 1], s1); else s0 = MFMA(kf[i], qf[i >> 1], s0);
      if (i + 4 < 16) kf[i + 4] = ldsfrag((kb ^ (32 * ((i + 4) >> 1))) + 8192 * (i & 1));
      SBAR();
    }
    __builtin_amdgcn_s_setprio(0);
    cur += SLOT; if (cur == ringhi) cur = ringlo;
    float mx0 = max3f(s0[0], s0[1], s1[0]), mx1 = max3f(s0[2], s0[3], s1[1]);
    mx0 = max3f(mx0, s1[2], s1[3]);
#pragma unroll
    for (int r = 4; r < 16; r += 4) { mx0 = max3f(mx0, s0[r], s0[r + 1]); mx1 = max3f(mx1, s0[r + 2], s0[r + 3]); mx0 = max3f(mx0, s1[r], s1[r + 1]); mx1 = max3f(mx1, s1[r + 2], s1[r + 3]); }
    mx0 = __builtin_fmaxf(mx0, mx1);
    { auto sw = __builtin_amdgcn_permlane32_swap(__float_as_uint(mx0), __float_as_uint(mx0), false, false);
      mx0 = __builtin_fmaxf(__uint_as_float(sw[0]), __uint_as_float(sw[1])); }
    if (__builtin_expect(__builtin_amdgcn_ballot_w64(!((mx0 - m) * SM_C <= SM_THR)) != 0, 0)) {
      const float mn = __builtin_fmaxf(m, mx0), alpha = __builtin_amdgcn_exp2f((m - mn) * SM_C);
      m = mn; l *= alpha;
#pragma unroll
      for (int c = 0; c < 4; ++c) acc[c] *= alpha;
    }
    const float nm = -m * SM_C;
    float ps0 = 0.f, ps1 = 0.f;
#pragma unroll
    for (int r = 0; r < 16; r += 2) { s0[r] = EXP2(s0[r]); s0[r + 1] = EXP2(s0[r + 1]); ps0 += s0[r]; ps1 += s0[r + 1]; }
    u32x4 pw[4];
    pw[0] = __builtin_bit_cast(u32x4, pack8(s0, 0)); pw[1] = __builtin_bit_cast(u32x4, pack8(s0, 8));
    PIN(pw[0]); PIN(pw[1]); PIN(ps0); PIN(ps1); SBAR();
    STAMP(12 + 4 * j);
    LBAR();
    STAMP(13 + 4 * j);
    SBAR();
    {
      const unsigned vb = cur + hi * 1024 + tr0; s16x4 vlo[16], vhi[16];
#pragma unroll
      for (int i = 0; i < 4; ++i) { vlo[i] = trread(vb ^ (64 * i)); vhi[i] = trread((vb ^ (64 * i)) + 2048); }
      SBAR();
#pragma unroll
      for (int i = 0; i < 16; ++i) {
        acc[i & 3] = MFMA(cat(vlo[i], vhi[i]), __builtin_bit_cast(bf16x8, pw[i >> 2]), acc[i & 3]);
        if (i + 4 < 16) { vlo[i + 4] = trread((vb ^ (64 * (i & 3))) + 4096 * ((i + 4) >> 2)); vhi[i + 4] = trread((vb ^ (64 * (i & 3))) + 4096 * ((i + 4) >> 2) + 2048); }
        if (i < 8) { s1[2 * i] = EXP2(s1[2 * i]); s1[2 * i + 1] = EXP2(s1[2 * i + 1]); ps0 += s1[2 * i]; ps1 += s1[2 * i + 1];
                     pw[2 + (i >> 2)][i & 3] = cvtpk(s1[2 * i], s1[2 * i + 1]); PIN(pw[2 + (i >> 2)]); PIN(ps0); PIN(ps1); }
        SBAR();
      }
    }
    l += ps0 + ps1;
    cur += SLOT; if (cur == ringhi) cur = ringlo;
  }
  if (!g) BAR();
  const float inv = 1.f / swap32_add(l);
  u16* op = Of + (((((size_t)br * NB + b) * 4 + wq) * 256 + head * 8) * 64 + lane) * 8;
#pragma unroll
  for (int c = 0; c < 4; ++c) {
    acc[c] *= inv;
    *(bf16x8*)(op + (2 * c) * 512) = pack8(acc[c], 0);
    *(bf16x8*)(op + (2 * c + 1) * 512) = pack8(acc[c], 8);
  }
  STAMP(42);
  STAMP(43); STAMPRT(63); STAMP_OUT(0);
}

constexpr unsigned OSLOT = 32768;
__global__ __launch_bounds__(NTHREADS, 3) void oproj_kernel(const u16* __restrict__ Of, const u16* __restrict__ woA, const u16* __restrict__ woB, float* __restrict__ P) {
  extern __shared__ __attribute__((aligned(16))) char lds[];
  const unsigned tid = threadIdx.x, lane = tid & 63;
  const unsigned w = rfl(tid >> 6);
  const unsigned xcd = blockIdx.x & 7, idx = blockIdx.x >> 3;
  const unsigned br = xcd >> 2, kq = xcd & 3, nb = idx & 3, b = idx >> 2;
  const unsigned ldsb = (unsigned)(size_t)lds;
  const unsigned l4 = lane >> 4, l15 = lane & 15;
  if (w >= 8) {
    const unsigned rot = ROTQ ? 2 * b + 4 * nb : 0;
    rsrc_t r; unsigned v, step4, so_mul, so_i0, so_i1, ldso;
    if (w == 8) {
      r = mkrsrc((br ? woB : woA) + (size_t)(kq * 1024) * DM + nb * 128, (1024 * DM - nb * 128) * 2);
      v = l4 * 1024 + 16 * (l15 ^ (l4 << 2)); step4 = 4096 - 1024; so_mul = 32 * 1024; so_i0 = 16 * 1024; so_i1 = 512 * 1024; ldso = 0;
    } else {
      r = mkrsrc(Of + (((size_t)br * NB + b) * 4 * 256 + kq * 64) * 512, 4 * 256 * 1024 - kq * 65536);
      v = lane * 16; step4 = 0; so_mul = 2048; so_i0 = 256 * 1024; so_i1 = 512 * 1024; ldso = 16384;
    }
    const bool isO = (w == 9);
    auto issue = [&](unsigned u, unsigned buf) {
      const unsigned uu = (u + rot) & 15;
#pragma unroll
      for (int i = 0; i < 4; ++i) { const unsigned s0 = uu * so_mul + (i & 1) * so_i0 + (i >> 1) * so_i1;
        const unsigned s2 = isO ? s0 + 32768 - 2048 : s0 + 2 * step4;
        dma4(r, v, s0, s0 + step4, s2, s2 + step4, buf + ldso + i * 4096); }
    };
    issue(0, ldsb); issue(1, ldsb + OSLOT); issue(2, ldsb + 2 * OSLOT);
    wait_vm<47>(); issue(3, ldsb + 3 * OSLOT);
    wait_bar<47>();
    unsigned buf = ldsb + 4 * OSLOT;
    for (unsigned u = 0; u < 12; ++u) { wait_bar<32>(); issue(u + 4, buf); buf += OSLOT; if (buf == ldsb + 5 * OSLOT) buf = ldsb; }
    wait_bar<32>(); wait_bar<16>(); wait_bar<0>(); BAR(); BAR();
    return;
  }
  const unsigned hi = lane >> 5, g = w >> 2, wq = w & 3;
  STAMP_DECL; STAMPRT(62); STAMP(0);
  const unsigned tq = l15 >> 2, tp = lane & 3, blk = l4 & 1;
  const unsigned oa = 16384 + wq * 4096 + g * 2048 + lane * 16, wo_ = g * 8192 + hi * 2048 + 256 * tq + 64 * tq + 32 * (tp & 1) + 16 * blk + 8 * (tp >> 1);
  f32x16 acc[4];
#pragma unroll
  for (int c = 0; c < 4; ++c) acc[c] = f32x16{};
  Frags F0, F1;
  BAR();
  STAMP(1);
  unsigned buf_ = ldsb;
  { const unsigned wb_ = buf_ + wo_; GREAD(F0, buf_ + oa, buf_ + oa + 1024); }
#define NEXTBUF() do { buf_ += OSLOT; if (buf_ == ldsb + 5 * OSLOT) buf_ = ldsb; } while (0)
  for (unsigned u = 0; u < 14; u += 2) {
    LBAR(); NEXTBUF();
    { const unsigned wb_ = buf_ + wo_; GSTEP(F0, F1, buf_ + oa, buf_ + oa + 1024); }
    LBAR(); NEXTBUF();
    { const unsigned wb_ = buf_ + wo_; GSTEP(F1, F0, buf_ + oa, buf_ + oa + 1024); }
    STAMP(2 + (u >> 1));
  }
  LBAR(); NEXTBUF();
  { const unsigned wb_ = buf_ + wo_; GSTEP(F0, F1, buf_ + oa, buf_ + oa + 1024); }
  LBAR();
  SBAR();
#pragma unroll
  for (int i = 0; i < 8; ++i) GMM(F1, i);
  STAMP(9);
  LAS f32x4* xw = (LAS f32x4*)(size_t)(ldsb + w * 8192 + lane * 16);
  const LAS f32x4* xr = (const LAS f32x4*)(size_t)(ldsb + (w ^ 4) * 8192 + lane * 16);
  float* pp = P + ((((((size_t)kq * 2 + br) * NB + b) * 4 + nb) * 4 + wq) * 4 + 2 * g) * 1024 + lane * 4;
#define XSEND(A, c) _Pragma("unroll") for (int q4 = 0; q4 < 4; ++q4) xw[((c) * 4 + q4) * 64] = f32x4{A[4 * q4], A[4 * q4 + 1], A[4 * q4 + 2], A[4 * q4 + 3]}
#define XRECV(A, c) _Pragma("unroll") for (int q4 = 0; q4 < 4; ++q4) { const f32x4 o = xr[((c) * 4 + q4) * 64]; \
    *(f32x4*)(pp + ((c) * 4 + q4) * 256) = f32x4{A[4 * q4] + o[0], A[4 * q4 + 1] + o[1], A[4 * q4 + 2] + o[2], A[4 * q4 + 3] + o[3]}; }
  if (g) { XSEND(acc[0], 0); XSEND(acc[1], 1); } else { XSEND(acc[2], 0); XSEND(acc[3], 1); }
  LBAR();
  STAMP(10);
  if (g) { XRECV(acc[2], 0); XRECV(acc[3], 1); } else { XRECV(acc[0], 0); XRECV(acc[1], 1); }
  STAMP(42);
  STAMP(43); STAMPRT(63); STAMP_OUT(1);
}

__global__ __launch_bounds__(256) void combine_kernel(const float* __restrict__ P, u16* __restrict__ out) {
  const unsigned t = blockIdx.x * 4 + (threadIdx.x >> 6), lane = threadIdx.x & 63;
  const unsigned c = t & 3, wq = (t >> 2) & 3, nb = (t >> 4) & 3, b = (t >> 6) & 7, br = t >> 9;
  const unsigned q = 32 * wq + (lane & 31), hi = lane >> 5;
  if (q >= SQ) return;
  const float* pp = P + (size_t)t * 1024 + lane * 4;
  f32x4 s[4];
#pragma unroll
  for (int q4 = 0; q4 < 4; ++q4) {
    f32x4 a = *(const f32x4*)(pp + q4 * 256);
#pragma unroll
    for (int k = 1; k < 4; ++k) a += *(const f32x4*)(pp + (size_t)k * 1024 * 1024 + q4 * 256);
    s[q4] = a;
  }
  u16* o = out + ((size_t)b * SQ + q) * 1024 + br * 512 + nb * 128 + c * 32 + hi * 16;
  *(u32x4*)o = u32x4{cvtpk(s[0][0], s[0][1]), cvtpk(s[0][2], s[0][3]), cvtpk(s[1][0], s[1][1]), cvtpk(s[1][2], s[1][3])};
  *(u32x4*)(o + 8) = u32x4{cvtpk(s[2][0], s[2][1]), cvtpk(s[2][2], s[2][3]), cvtpk(s[3][0], s[3][1]), cvtpk(s[3][2], s[3][3])};
}

extern "C" void kernel_launch(void* const* d_in, const int* in_sizes, int n_in, void* d_out, int out_size, void* d_ws, size_t ws_size, hipStream_t stream) {
  const u16* xA = (const u16*)d_in[0]; const u16* kA = (const u16*)d_in[1]; const u16* vA = (const u16*)d_in[2]; const u16* wqA = (const u16*)d_in[3]; const u16* woA = (const u16*)d_in[4];
  const u16* xB = (const u16*)d_in[5]; const u16* kB = (const u16*)d_in[6]; const u16* vB = (const u16*)d_in[7]; const u16* wqB = (const u16*)d_in[8]; const u16* woB = (const u16*)d_in[9];
  u16* Of = (u16*)((char*)d_ws + WS_O);
  float* P = (float*)((char*)d_ws + WS_P);
  (void)hipFuncSetAttribute((const void*)attn_kernel, hipFuncAttributeMaxDynamicSharedMemorySize, LDS_BYTES);
  (void)hipFuncSetAttribute((const void*)oproj_kernel, hipFuncAttributeMaxDynamicSharedMemorySize, LDS_BYTES);
  attn_kernel<<<256, NTHREADS, LDS_BYTES, stream>>>(xA, kA, vA, wqA, xB, kB, vB, wqB, Of);
  oproj_kernel<<<256, NTHREADS, LDS_BYTES, stream>>>(Of, woA, woB, P);
  combine_kernel<<<256, 256, 0, stream>>>(P, (u16*)d_out);
}
```

```cpp
#include <hip/hip_runtime.h>
#include <hip/hip_bf16.h>

typedef __bf16 bf16x8 __attribute__((ext_vector_type(8)));
typedef __bf16 bf16x2 __attribute__((ext_vector_type(2)));
typedef short s16x4 __attribute__((ext_vector_type(4)));
typedef short s16x8 __attribute__((ext_vector_type(8)));
typedef float f32x16 __attribute__((ext_vector_type(16)));
typedef float f32x4 __attribute__((ext_vector_type(4)));
typedef float f32x2 __attribute__((ext_vector_type(2)));
typedef unsigned u32x4 __attribute__((ext_vector_type(4)));
typedef unsigned u32x2 __attribute__((ext_vector_type(2)));
typedef unsigned short u16;
#define LAS __attribute__((address_space(3)))
#define DI __device__ __forceinline__
#define MFMA(a, b, c) __builtin_amdgcn_mfma_f32_32x32x16_bf16((a), (b), (c), 0, 0, 0)

constexpr int NB = 8, SQ = 112, DM = 512, NH = 32, SK = 512, DH = 128, HD = NH * DH;
constexpr unsigned SLOT = 16384;
constexpr int LDS_BYTES = 163840;
constexpr float SM_C = 0.08838834764831845f * 1.4426950408889634f;
constexpr float SM_THR = 8.f;

constexpr size_t WS_SY = 0;
constexpr size_t WS_O = 262144;
constexpr size_t WS_O_BYTES = (size_t)2 * NB * 4 * 256 * 1024;
constexpr size_t WS_P = WS_O + WS_O_BYTES;

DI unsigned rfl(unsigned x) { return __builtin_amdgcn_readfirstlane(x); }
DI unsigned cvtpk(float lo, float hi) { f32x2 v = {lo, hi}; bf16x2 b = __builtin_convertvector(v, bf16x2); return __builtin_bit_cast(unsigned, b); }
DI bf16x8 pack8(const f32x16& x, int base) {
  u32x4 w = {cvtpk(x[base + 0], x[base + 1]), cvtpk(x[base + 2], x[base + 3]), cvtpk(x[base + 4], x[base + 5]), cvtpk(x[base + 6], x[base + 7])};
  return __builtin_bit_cast(bf16x8, w);
}
DI s16x4 trread(unsigned a) { return __builtin_amdgcn_ds_read_tr16_b64_v4i16((LAS s16x4*)(size_t)a); }
DI bf16x8 cat(s16x4 lo, s16x4 hi) { s16x8 r = __builtin_shufflevector(lo, hi, 0, 1, 2, 3, 4, 5, 6, 7); return __builtin_bit_cast(bf16x8, r); }
DI bf16x8 ldsfrag(unsigned a) { return *(const LAS bf16x8*)(size_t)a; }
DI float swap32_add(float x) { auto r = __builtin_amdgcn_permlane32_swap(__float_as_uint(x), __float_as_uint(x), false, false); return __uint_as_float(r[0]) + __uint_as_float(r[1]); }
DI float max3f(float a, float b, float c) { return __builtin_fmaxf(__builtin_fmaxf(a, b), c); }

typedef __amdgpu_buffer_rsrc_t rsrc_t;
DI rsrc_t mkrsrc(const void* p, unsigned bytes) { return __builtin_amdgcn_make_buffer_rsrc((void*)p, (short)0, (int)bytes, 0x00020000); }

#define KVPOL "nt"
DI void dma4(rsrc_t r, unsigned voff, unsigned s0, unsigned s1, unsigned s2, unsigned s3, unsigned m0v) {
  unsigned keep;
  asm volatile("s_mov_b32 %0, m0\n\ts_mov_b32 m0, %7\n\ts_nop 0\n\t"
               "buffer_load_dwordx4 %1, %2, %3 offen lds\n\t"
               "buffer_load_dwordx4 %1, %2, %4 offen offset:1024 lds\n\t"
               "buffer_load_dwordx4 %1, %2, %5 offen offset:2048 lds\n\t"
               "buffer_load_dwordx4 %1, %2, %6 offen offset:3072 lds\n\t"
               "s_mov_b32 m0, %0"
               : "=&s"(keep) : "v"(voff), "s"(r), "s"(s0), "s"(s1), "s"(s2), "s"(s3), "s"(m0v) : "memory");
}
DI void dma3(rsrc_t r, unsigned voff, unsigned s0, unsigned s1, unsigned s2, unsigned m0v) {
  unsigned keep;
  asm volatile("s_mov_b32 %0, m0\n\ts_mov_b32 m0, %6\n\ts_nop 0\n\t"
               "buffer_load_dwordx4 %1, %2, %3 offen lds\n\t"
               "buffer_load_dwordx4 %1, %2, %4 offen offset:1024 lds\n\t"
               "buffer_load_dwordx4 %1, %2, %5 offen offset:2048 lds\n\t"
               "s_mov_b32 m0, %0"
               : "=&s"(keep) : "v"(voff), "s"(r), "s"(s0), "s"(s1), "s"(s2), "s"(m0v) : "memory");
}
DI void dma4_kv(rsrc_t r, unsigned v0, unsigned v1, unsigned v2, unsigned v3, unsigned soff, unsigned m0v) {
  unsigned keep;
  asm volatile("s_mov_b32 %0, m0\n\ts_mov_b32 m0, %7\n\ts_nop 0\n\t"
               "buffer_load_dwordx4 %1, %5, %6 offen " KVPOL " lds\n\t"
               "buffer_load_dwordx4 %2, %5, %6 offen offset:1024 " KVPOL " lds\n\t"
               "buffer_load_dwordx4 %3, %5, %6 offen offset:2048 " KVPOL " lds\n\t"
               "buffer_load_dwordx4 %4, %5, %6 offen offset:3072 " KVPOL " lds\n\t"
               "s_mov_b32 m0, %0"
               : "=&s"(keep) : "v"(v0), "v"(v1), "v"(v2), "v"(v3), "s"(r), "s"(soff), "s"(m0v) : "memory");
}
template <int N> DI void wait_bar() { asm volatile("s_waitcnt vmcnt(%0) lgkmcnt(0)\n\ts_barrier" :: "i"(N) : "memory"); }
template <int N> DI void wait_vm() { asm volatile("s_waitcnt vmcnt(%0)" :: "i"(N) : "memory"); }
#define SBAR() __builtin_amdgcn_sched_barrier(0)
#define BAR() asm volatile("s_barrier" ::: "memory")
#define LBAR() asm volatile("s_waitcnt lgkmcnt(0)\n\ts_barrier" ::: "memory")
#define PIN(x) asm volatile("" : "+v"(x))

#define STAMP_DECL
#define STAMP(i)
#define STAMPRT(i)
#define STAMP_OUT(k)
#define STAMP2(i)
#define PIN2(x)
#define ROTQ 1
#define ROTK 1
#define KV_AT 0

constexpr int NTHREADS = 768;
constexpr unsigned PBUF = 32768;
constexpr unsigned LDS_R0 = 6 * SLOT, LDS_R1 = 8 * SLOT, LDS_X = 4 * SLOT;

DI void dma2(rsrc_t r, unsigned v0, unsigned v1, unsigned s0, unsigned s1, unsigned m0v) {
  unsigned keep;
  asm volatile("s_mov_b32 %0, m0\n\ts_mov_b32 m0, %6\n\ts_nop 0\n\t"
               "buffer_load_dwordx4 %1, %3, %4 offen lds\n\t"
               "buffer_load_dwordx4 %2, %3, %5 offen offset:1024 lds\n\t"
               "s_mov_b32 m0, %0"
               : "=&s"(keep) : "v"(v0), "v"(v1), "s"(r), "s"(s0), "s"(s1), "s"(m0v) : "memory");
}
#define CODE_KB 0
#define KV_A 0
#define KV_B 4
typedef __attribute__((address_space(1))) unsigned gu32;
#define RLX_AGENT __ATOMIC_RELAXED, __HIP_MEMORY_SCOPE_AGENT
#define VM0() asm volatile("s_waitcnt vmcnt(0)" ::: "memory")
DI unsigned wo_slot(unsigned e) { return (unsigned)((0x3982103254769876ull >> (4 * e)) & 15) * SLOT; }

__global__ __launch_bounds__(NTHREADS, 3) void fused_kernel(const u16* __restrict__ xA, const u16* __restrict__ kA, const u16* __restrict__ vA, const u16* __restrict__ wqA, const u16* __restrict__ woA,
                                                            const u16* __restrict__ xB, const u16* __restrict__ kB, const u16* __restrict__ vB, const u16* __restrict__ wqB, const u16* __restrict__ woB,
                                                            char* __restrict__ ws, u16* __restrict__ out) {
  extern __shared__ __attribute__((aligned(16))) char lds[];
  unsigned long long pc0; asm volatile("s_getpc_b64 %0" : "=s"(pc0));
  const unsigned tid = threadIdx.x, lane = tid & 63;
  const unsigned w = rfl(tid >> 6);
  const unsigned xcd = blockIdx.x & 7, idx = blockIdx.x >> 3;
  const unsigned br = xcd >> 2, oct = xcd & 3, hpl = idx & 3, hp = oct * 4 + hpl, b = idx >> 2;
  const unsigned ldsb = (unsigned)(size_t)lds;
  const unsigned l4 = lane >> 4, l15 = lane & 15;
  gu32* const sy = (gu32*)(ws + WS_SY);

  if (w >= 10) {
    const unsigned rotq = ROTQ ? b + 2 * hpl : 0;
    const bool isx = w == 11;
    {
      const rsrc_t r = isx ? mkrsrc((br ? xB : xA) + (size_t)b * SQ * DM, SQ * DM * 2) : mkrsrc((br ? wqB : wqA) + (size_t)hp * 256, (DM * HD - hp * 256) * 2);
      const unsigned v0 = isx ? (lane >> 3) * 1024 + 16 * ((lane & 7) ^ l4) : l4 * 8192 + 16 * (l15 ^ (l4 << 2));
      const unsigned v1 = isx ? (lane >> 3) * 1024 + 16 * ((lane & 7) ^ (4 + l4)) : v0;
      const unsigned pstep = isx ? 8192 : 32768, ustep = isx ? 128 : 64 * 8192, gstep = isx ? 0 : 256, npair = isx ? 7 : 8, lo = isx ? 16384 : 0;
      auto issue = [&](unsigned gg, unsigned u, unsigned buf) {
        unsigned s0 = gg * gstep + ((u + rotq) & 7) * ustep, m0 = buf + lo;
#pragma nounroll
        for (unsigned i = 0; i < npair; ++i) { dma2(r, v0, v1, s0, s0 + pstep - 1024, m0); s0 += 2 * pstep; m0 += 2048; }
      };
      issue(0, 0, ldsb + LDS_R0); issue(0, 1, ldsb + LDS_R1);
      unsigned buf = ldsb + LDS_X;
#pragma nounroll
      for (unsigned i = 0; i < 6; ++i) { wait_bar<14>(); issue(0, i + 2, buf); buf += PBUF; if (buf == ldsb + 10 * SLOT) buf = ldsb + LDS_X; }
      wait_bar<14>(); wait_bar<0>(); issue(1, 0, ldsb + LDS_R0);
#pragma nounroll
      for (unsigned j = 0; j < 7; ++j) {
        BAR(); issue(1, j + 1, ldsb + LDS_R0 + ((j + 1) & 1) * PBUF);
        wait_bar<14>();
      }
      BAR(); wait_bar<0>();
    }
    const rsrc_t rO = mkrsrc((br ? woB : woA) + (size_t)(oct * 1024) * DM + hpl * 128, (1024 * DM - hpl * 128) * 2);
    const unsigned vO = l4 * 1024 + 16 * (l15 ^ (l4 << 2));
    auto issueO = [&](unsigned e) {
      const unsigned t = e >> 1, hl = 2 * ((hpl + t) & 3) + (t >> 2);
      const unsigned s0 = (hl * 128 + (e & 1) * 64 + (w - 10) * 32) * 1024, m0 = ldsb + wo_slot(e) + (w - 10) * 8192;
      dma4(rO, vO, s0, s0 + 3072, s0 + 6144, s0 + 9216, m0);
      dma4(rO, vO, s0 + 16384, s0 + 16384 + 3072, s0 + 16384 + 6144, s0 + 16384 + 9216, m0 + 4096);
    };
    const unsigned mbR = xcd + 8 * (4 * b + (lane & 3)), mbC = 4 * br + (lane & 3) + 8 * idx;
    unsigned nonce = 0, echo = 0;
    auto refresh = [&](unsigned stage, bool wait) {
      const unsigned me = stage < 2 ? hpl : oct, mb = stage < 2 ? mbR : mbC;
#pragma nounroll
      for (unsigned spin = 0; spin < (1u << 16); ++spin) {
        const unsigned ack = __hip_atomic_load(sy + blockIdx.x * 128 + 4 * stage + (lane & 3), RLX_AGENT);
        const unsigned e2 = __hip_atomic_load(sy + mb * 128 + 64, RLX_AGENT);
        if (lane < 4 && lane != me && e2 != echo) __hip_atomic_store(sy + mb * 128 + 4 * stage + me, e2, RLX_AGENT);
        echo = e2;
        if (!wait || __builtin_amdgcn_ballot_w64(ack != nonce && (lane & 3) != me) == 0) break;
        __builtin_amdgcn_s_sleep(1);
      }
      __builtin_amdgcn_fence(__ATOMIC_ACQUIRE, "wavefront");
    };
    unsigned echoC = 0, ack0 = 0;
    BAR(); issueO(0); issueO(1); issueO(2); issueO(3);
    if (isx) { echo = __hip_atomic_load(sy + mbR * 128 + 64, RLX_AGENT); echoC = __hip_atomic_load(sy + mbC * 128 + 64, RLX_AGENT); }
    BAR(); BAR();
    wait_bar<16>(); BAR();
    BAR();
    BAR();
    if (isx) { nonce = __builtin_amdgcn_readlane(echo, hpl); if (lane < 4 && lane != hpl) __hip_atomic_store(sy + mbR * 128 + hpl, echo, RLX_AGENT); }
    issueO(4); issueO(5);
    BAR(); BAR();
    if (isx) ack0 = __hip_atomic_load(sy + blockIdx.x * 128 + (lane & 3), RLX_AGENT);
    BAR(); BAR();
    BAR();
    if (isx) { if (__builtin_amdgcn_ballot_w64(ack0 != nonce && (lane & 3) != hpl) != 0) refresh(0, true); }
    issueO(8);
    BAR();
    BAR(); issueO(9); issueO(6);
    BAR();
    wait_bar<24>(); issueO(7); issueO(10);
    BAR(); issueO(13);
    wait_bar<32>(); issueO(11); issueO(14);
    if (isx) refresh(0, false);
    wait_bar<32>();
    if (isx) { if (lane < 4 && lane != hpl) __hip_atomic_store(sy + mbR * 128 + 4 + hpl, echo, RLX_AGENT); }
    issueO(12); issueO(15);
    if (isx) refresh(1, true);
    wait_bar<0>();
    if (!isx) return;
    BAR();
    BAR();
    echo = echoC;
    if (lane < 4 && lane != oct) __hip_atomic_store(sy + mbC * 128 + 8 + oct, echo, RLX_AGENT);
    refresh(2, true);
    BAR();
    refresh(2, false);
    return;
  }
  if (w >= 8) {
    const unsigned kbase = l4 * 256 + 16 * (l15 ^ l4);
    const unsigned vV = l4 * 256 + 16 * (l15 ^ (l4 << 2));
    const size_t kvoff = ((size_t)b * NH + 2 * hp) * SK * DH;
    const rsrc_t rK = mkrsrc((br ? kB : kA) + kvoff, 2 * SK * DH * 2);
    const rsrc_t rV = mkrsrc((br ? vB : vA) + kvoff, 2 * SK * DH * 2);
    const unsigned rotk = ROTK ? b + hp : 0, half = (w - 8) * 8192;
    unsigned slot = ldsb + half;
    auto issue = [&](unsigned n) {
      const unsigned so = (n >> 4) * 8 * SLOT + (((n >> 1) + rotk) & 7) * SLOT + half;
      if (n & 1) { dma4_kv(rV, vV, vV, vV, vV, so, slot); dma4_kv(rV, vV, vV, vV, vV, so + 4096, slot + 4096); }
      else { dma4_kv(rK, kbase, kbase ^ 64, kbase ^ 128, kbase ^ 192, so, slot); dma4_kv(rK, kbase, kbase ^ 64, kbase ^ 128, kbase ^ 192, so + 4096, slot + 4096); }
      slot += SLOT; if (slot >= ldsb + 6 * SLOT) slot = ldsb + half;
    };
    if (KV_A < 0) { issue(0); issue(1); }
#pragma nounroll
    for (int i = 0; i < 8; ++i) {
      BAR();
      if (i == KV_A || i == KV_B || i == 6) { const unsigned n = i == KV_A ? 0 : i == KV_B ? 2 : 4; issue(n); issue(n + 1); }
    }
    wait_bar<40>();
    wait_bar<32>(); issue(6);
#pragma nounroll
    for (unsigned J = 1; J < 13; ++J) {
      wait_bar<32>();
      wait_bar<24>(); issue(2 * J + 5); issue(2 * J + 6);
    }
    wait_bar<32>(); wait_bar<24>(); issue(31);
    wait_bar<24>(); wait_bar<16>();
    wait_bar<8>(); wait_bar<0>();
    return;
  }

  const unsigned r32 = lane & 31, hi = lane >> 5, g = w >> 2, wq = w & 3;
  STAMP_DECL; STAMPRT(62); STAMP(0);
  const unsigned tq = l15 >> 2, tp = lane & 3, blk = l4 & 1;
  const unsigned tr0 = 256 * tq + 64 * tq + 32 * blk + 16 * (tp & 1) + 8 * (tp >> 1);
  const unsigned xrow = (32 * wq + r32 < SQ) ? 32 * wq + r32 : 32 * wq + r32 - 16;
  const unsigned xa0 = 16384 + 128 * xrow + 16 * (hi ^ ((xrow >> 1) & 7));
  const unsigned ka0 = 256 * r32 + 16 * (hi ^ (r32 & 15));

  f32x16 acc[4];
#pragma unroll
  for (int c = 0; c < 4; ++c) acc[c] = f32x16{};
  if (g) {
    unsigned old = 0;
    if (w == 4) old = __hip_atomic_load(sy + blockIdx.x * 128 + (lane < 12 ? lane : 0), RLX_AGENT);
    BAR(); BAR(); BAR();
    if (w == 4) {
      unsigned n = rfl(old) + 1;
      while (__builtin_amdgcn_ballot_w64(old == n) != 0) ++n;
      if (lane == 0) __hip_atomic_store(sy + blockIdx.x * 128 + 64, n, RLX_AGENT);
    }
    BAR(); BAR(); BAR(); BAR();
    VM0();
    BAR();
  }
  {
    unsigned buf = ldsb + LDS_R0;
#pragma nounroll
    for (unsigned u = 0; u < 8; ++u) {
      if (g) LBAR();
      LBAR();
      STAMP(1 + u);
      const unsigned wb = buf + hi * 2048 + tr0, xb = buf + xa0;
      bf16x8 xf[4]; s16x4 lo[16], hi_[16];
      SBAR();
      xf[0] = ldsfrag(xb);
#pragma unroll
      for (int i = 0; i < 4; ++i) { lo[i] = trread(wb ^ (64 * i)); hi_[i] = trread((wb ^ (64 * i)) + 1024); }
      SBAR();
#pragma unroll
      for (int i = 0; i < 16; ++i) {
        acc[i & 3] = MFMA(cat(lo[i], hi_[i]), xf[i >> 2], acc[i & 3]);
        if (i + 4 < 16) { if ((i & 3) == 0) xf[(i >> 2) + 1] = ldsfrag(xb ^ (32 * ((i >> 2) + 1)));
                          lo[i + 4] = trread((wb ^ (64 * (i & 3))) + 4096 * ((i + 4) >> 2)); hi_[i + 4] = trread((wb ^ (64 * (i & 3))) + 4096 * ((i + 4) >> 2) + 1024); }
        SBAR();
      }
      buf += PBUF; if (buf == ldsb + 10 * SLOT) buf = ldsb + (g ? LDS_R0 : LDS_X);
    }
  }
  bf16x8 qf[8];
#pragma unroll
  for (int c = 0; c < 4; ++c) { qf[2 * c] = pack8(acc[c], 0); qf[2 * c + 1] = pack8(acc[c], 8); }

#pragma unroll
  for (int c = 0; c < 4; ++c) acc[c] = f32x16{};
  float m = -__builtin_inff(), l = 0.f;
  unsigned cur = ldsb + (g ? 4 * SLOT : 0);
  STAMP(9);
#define EXP2(v) __builtin_amdgcn_exp2f(__builtin_fmaf((v), SM_C, nm))
#define NEXTSLOT() do { cur += SLOT; if (cur == ldsb + 6 * SLOT) cur = ldsb; } while (0)
  bf16x8 kf[16];
  LBAR();
#pragma unroll
  for (int i = 0; i < 4; ++i) kf[i] = ldsfrag(((cur + ka0) ^ (32 * (i >> 1))) + 8192 * (i & 1));
#pragma nounroll
  for (unsigned j = 0; j < 8; ++j) {
    STAMP(10 + 4 * j);
    f32x16 s0 = f32x16{}, s1 = f32x16{};
    {
      const unsigned kb = cur + ka0;
      SBAR();
#pragma unroll
      for (int i = 0; i < 16; ++i) {
        if (i & 1) s1 = MFMA(kf[i], qf[i >> 1], s1); else s0 = MFMA(kf[i], qf[i >> 1], s0);
        if (i + 4 < 16) kf[i + 4] = ldsfrag((kb ^ (32 * ((i + 4) >> 1))) + 8192 * (i & 1));
        SBAR();
      }
    }
    NEXTSLOT();
    STAMP2(11 + 4 * j);
    LBAR();
    STAMP2(12 + 4 * j);
    SBAR();
    float mx0 = max3f(s0[0], s0[1], s0[2]), mx1 = max3f(s0[8], s0[9], s0[10]), mx2 = max3f(s1[0], s1[1], s1[2]), mx3 = max3f(s1[8], s1[9], s1[10]);
    mx0 = max3f(mx0, s0[3], s0[4]); mx1 = max3f(mx1, s0[11], s0[12]); mx2 = max3f(mx2, s1[3], s1[4]); mx3 = max3f(mx3, s1[11], s1[12]);
    mx0 = max3f(mx0, s0[5], s0[6]); mx1 = max3f(mx1, s0[13], s0[14]); mx2 = max3f(mx2, s1[5], s1[6]); mx3 = max3f(mx3, s1[13], s1[14]);
    mx0 = max3f(mx0, s0[7], s0[15]); mx2 = max3f(mx2, s1[7], s1[15]);
    mx0 = max3f(mx0, mx1, mx3); mx0 = __builtin_fmaxf(mx0, mx2);
    { auto sw = __builtin_amdgcn_permlane32_swap(__float_as_uint(mx0), __float_as_uint(mx0), false, false);
      mx0 = __builtin_fmaxf(__uint_as_float(sw[0]), __uint_as_float(sw[1])); }
    if (__builtin_expect(__builtin_amdgcn_ballot_w64(!((mx0 - m) * SM_C <= SM_THR)) != 0, 0)) {
      const float mn = __builtin_fmaxf(m, mx0), alpha = __builtin_amdgcn_exp2f((m - mn) * SM_C);
      m = mn; l *= alpha;
#pragma unroll
      for (int c = 0; c < 4; ++c) acc[c] *= alpha;
    }
    const float nm = -m * SM_C;
    const f32x2 nm2 = {nm, nm}, c2 = {SM_C, SM_C};
    f32x2 ps0 = {0.f, 0.f}, ps1 = {0.f, 0.f};
    u32x4 pw[4]; s16x4 vlo[16], vhi[16];
    const unsigned vb = cur + hi * 1024 + tr0;
#define SM_FMA(S, o) _Pragma("unroll") for (int r = (o); r < (o) + 8; r += 2) { f32x2 t = {S[r], S[r + 1]}; t = __builtin_elementwise_fma(t, c2, nm2); S[r] = t[0]; S[r + 1] = t[1]; }
#define SM_EXP(S, o) _Pragma("unroll") for (int r = (o); r < (o) + 8; ++r) S[r] = __builtin_amdgcn_exp2f(S[r]);
#define SM_SUM(S, o, W) { ps0 += f32x2{S[(o)], S[(o) + 1]}; ps1 += f32x2{S[(o) + 2], S[(o) + 3]}; W = __builtin_bit_cast(u32x4, pack8(S, (o))); ps0 += f32x2{S[(o) + 4], S[(o) + 5]}; ps1 += f32x2{S[(o) + 6], S[(o) + 7]}; }
    SBAR();
    SM_FMA(s0, 0); SM_FMA(s0, 8); SBAR();
    SM_EXP(s0, 0); SM_FMA(s1, 0); SBAR();
    SM_EXP(s0, 8); SM_FMA(s1, 8); SM_SUM(s0, 0, pw[0]); PIN(pw[0]); SBAR();
    SM_EXP(s1, 0); SM_SUM(s0, 8, pw[1]); PIN(pw[1]); SBAR();
#pragma unroll
    for (int i = 0; i < 4; ++i) { vlo[i] = trread(vb ^ (64 * i)); vhi[i] = trread((vb ^ (64 * i)) + 2048); }
    SM_EXP(s1, 8); SM_SUM(s1, 0, pw[2]); PIN(pw[2]); SBAR();
    SM_SUM(s1, 8, pw[3]);
    ps0 += ps1; l += ps0[0] + ps0[1];
    PIN(pw[3]); PIN(l); SBAR();
    STAMP2(13 + 4 * j);
    LBAR();
    SBAR();
    NEXTSLOT();
    {
      const unsigned kb = cur + ka0;
#pragma unroll
      for (int i = 0; i < 16; ++i) {
        acc[i & 3] = MFMA(cat(vlo[i], vhi[i]), __builtin_bit_cast(bf16x8, pw[i >> 2]), acc[i & 3]);
        if (i + 4 < 16) { vlo[i + 4] = trread((vb ^ (64 * (i & 3))) + 4096 * ((i + 4) >> 2)); vhi[i + 4] = trread((vb ^ (64 * (i & 3))) + 4096 * ((i + 4) >> 2) + 2048); }
        else kf[i - 12] = ldsfrag((kb ^ (32 * ((i - 12) >> 1))) + 8192 * (i & 1));
        SBAR();
      }
    }
  }
  STAMP(46);
  LBAR();
  const rsrc_t rOf = mkrsrc(ws + WS_O + ((((size_t)br * NB + b) * 4 + wq) * 256 + oct * 64) * 1024, 65536);
  bf16x8 of[8];
  {
    const float inv = 1.f / swap32_add(l);
#pragma unroll
    for (int c = 0; c < 4; ++c) { acc[c] *= inv; of[2 * c] = pack8(acc[c], 0); of[2 * c + 1] = pack8(acc[c], 8); }
#pragma unroll
    for (int s = 0; s < 8; ++s) __builtin_amdgcn_raw_buffer_store_b128(__builtin_bit_cast(u32x4, of[s]), rOf, lane * 16, (2 * hpl + g) * 8192 + s * 1024, 16);
  }
  STAMP(47);
#pragma unroll
  for (int c = 0; c < 4; ++c) acc[c] = f32x16{};
  const unsigned tro = ldsb + hi * 2048 + 256 * tq + 64 * tq + 32 * (tp & 1) + 16 * blk + 8 * (tp >> 1);
#define NBAR(n) do { _Pragma("nounroll") for (unsigned i_ = (n); i_ > 0; --i_) LBAR(); } while (0)
#define OITEM(F, fo, e) do { const unsigned wb = wo_slot(e) + tro; s16x4 lo[16], hi_[16]; SBAR(); \
    _Pragma("unroll") for (int i = 0; i < 4; ++i) { lo[i] = trread(wb ^ (64 * i)); hi_[i] = trread((wb ^ (64 * i)) + 1024); } \
    SBAR(); \
    _Pragma("unroll") for (int i = 0; i < 16; ++i) {     \
      acc[i & 3] = MFMA(cat(lo[i], hi_[i]), F[(fo) + (i >> 2)], acc[i & 3]); \
      if (i + 4 < 16) { lo[i + 4] = trread((wb ^ (64 * (i & 3))) + 4096 * ((i + 4) >> 2)); hi_[i + 4] = trread((wb ^ (64 * (i & 3))) + 4096 * ((i + 4) >> 2) + 1024); } \
      SBAR(); } } while (0)
  NBAR(g ? 0 : 2); OITEM(of, 0, 8 * g);
  NBAR(g ? 0 : 2); OITEM(of, 4, 8 * g + 1);
  STAMP(49);
  wait_bar<0>();
#pragma nounroll
  for (unsigned r = g ? 2 : 0; r < 3; ++r) {
    NBAR(r == 0 ? 6 : r == 1 ? 0 : 1);
    const unsigned e0 = r == 2 ? 10 + 3 * g : 2 + 3 * r;
    bf16x8 pf[12];
#pragma unroll
    for (int k = 0; k < 3; ++k) {
      const unsigned e = e0 + k, t = e >> 1, so = (2 * ((hpl + t) & 3) + (t >> 2)) * 8192 + (e & 1) * 4096;
#pragma unroll
      for (int s = 0; s < 4; ++s) pf[4 * k + s] = __builtin_bit_cast(bf16x8, __builtin_amdgcn_raw_buffer_load_b128(rOf, lane * 16, so + s * 1024, 16));
    }
    STAMP(50 + 2 * r);
    NBAR(r == 0 ? 3 : r == 1 ? 1 : 0); OITEM(pf, 0, e0);
    NBAR(r == 0);                      OITEM(pf, 4, e0 + 1);
    NBAR(r == 0);                      OITEM(pf, 8, e0 + 2);
    STAMP(51 + 2 * r);
  }
  {
    LAS f32x4* xw = (LAS f32x4*)(size_t)(ldsb + 4 * SLOT + w * 8192 + lane * 16);
    const LAS f32x4* xr = (const LAS f32x4*)(size_t)(ldsb + 4 * SLOT + (w ^ 4) * 8192 + lane * 16);
#define XSEND(A, c) _Pragma("unroll") for (int q4 = 0; q4 < 4; ++q4) xw[((c) * 4 + q4) * 64] = f32x4{A[4 * q4], A[4 * q4 + 1], A[4 * q4 + 2], A[4 * q4 + 3]}
    if (g) { XSEND(acc[0], 0); XSEND(acc[1], 1); acc[0] = acc[2]; acc[1] = acc[3]; } else { XSEND(acc[2], 0); XSEND(acc[3], 1); }
    LBAR();
#pragma unroll
    for (int c = 0; c < 2; ++c)
#pragma unroll
      for (int q4 = 0; q4 < 4; ++q4) { const f32x4 o = xr[(c * 4 + q4) * 64];
#pragma unroll
        for (int k = 0; k < 4; ++k) acc[c][4 * q4 + k] += o[k]; }
  }
  STAMP(57);
  const rsrc_t rP = mkrsrc(ws + WS_P + (((size_t)br * NB + b) * 4 + hpl) * 262144, 262144);
#pragma unroll
  for (int c = 0; c < 2; ++c) {
    if (2 * g + c != oct) {
#pragma unroll
      for (int q4 = 0; q4 < 4; ++q4)
        __builtin_amdgcn_raw_buffer_store_b128(__builtin_bit_cast(u32x4, f32x4{acc[c][4 * q4], acc[c][4 * q4 + 1], acc[c][4 * q4 + 2], acc[c][4 * q4 + 3]}), rP, lane * 16, (((2 * g + c) * 4 + oct) * 4 + wq) * 4096 + q4 * 1024, 16);
    }
  }
  if (oct & 1) acc[0] = acc[1];
  VM0();
  STAMP(58);
  BAR();
  if (g != (oct >> 1)) return;
  BAR();
  STAMP(59);
  if (g == (oct >> 1)) {
    f32x4 pv[12];
#pragma unroll
    for (int k = 0; k < 3; ++k)
#pragma unroll
      for (int q4 = 0; q4 < 4; ++q4)
        pv[4 * k + q4] = __builtin_bit_cast(f32x4, __builtin_amdgcn_raw_buffer_load_b128(rP, lane * 16, ((oct * 4 + ((oct + 1 + k) & 3)) * 4 + wq) * 4096 + q4 * 1024, 16));
#pragma unroll
    for (int k = 0; k < 3; ++k)
#pragma unroll
      for (int r = 0; r < 16; ++r) acc[0][r] += pv[4 * k + (r >> 2)][r & 3];
    const unsigned q = 32 * wq + r32;
    if (q < SQ) {
      u16* o = out + ((size_t)b * SQ + q) * 1024 + br * 512 + hpl * 128 + oct * 32 + hi * 16;
      *(bf16x8*)o = pack8(acc[0], 0);
      *(bf16x8*)(o + 8) = pack8(acc[0], 8);
    }
  }
  STAMP(48); STAMPRT(63); STAMP_OUT(0);
}

extern "C" void kernel_launch(void* const* d_in, const int* in_sizes, int n_in, void* d_out, int out_size, void* d_ws, size_t ws_size, hipStream_t stream) {
  const u16* xA = (const u16*)d_in[0]; const u16* kA = (const u16*)d_in[1]; const u16* vA = (const u16*)d_in[2]; const u16* wqA = (const u16*)d_in[3]; const u16* woA = (const u16*)d_in[4];
  const u16* xB = (const u16*)d_in[5]; const u16* kB = (const u16*)d_in[6]; const u16* vB = (const u16*)d_in[7]; const u16* wqB = (const u16*)d_in[8]; const u16* woB = (const u16*)d_in[9];
  (void)hipFuncSetAttribute((const void*)fused_kernel, hipFuncAttributeMaxDynamicSharedMemorySize, LDS_BYTES);
  fused_kernel<<<256, NTHREADS, LDS_BYTES, stream>>>(xA, kA, vA, wqA, woA, xB, kB, vB, wqB, woB, (char*)d_ws, (u16*)d_out);
}
```

```cpp
#include <hip/hip_runtime.h>
#include <hip/hip_bf16.h>

typedef __bf16 bf16x8 __attribute__((ext_vector_type(8)));
typedef __bf16 bf16x2 __attribute__((ext_vector_type(2)));
typedef short s16x4 __attribute__((ext_vector_type(4)));
typedef short s16x8 __attribute__((ext_vector_type(8)));
typedef float f32x16 __attribute__((ext_vector_type(16)));
typedef float f32x4 __attribute__((ext_vector_type(4)));
typedef float f32x2 __attribute__((ext_vector_type(2)));
typedef unsigned u32x4 __attribute__((ext_vector_type(4)));
typedef unsigned u32x2 __attribute__((ext_vector_type(2)));
typedef unsigned short u16;
#define LAS __attribute__((address_space(3)))
#define DI __device__ __forceinline__
#define MFMA(a, b, c) __builtin_amdgcn_mfma_f32_32x32x16_bf16((a), (b), (c), 0, 0, 0)

constexpr int NB = 8, SQ = 112, DM = 512, NH = 32, SK = 512, DH = 128, HD = NH * DH;
constexpr unsigned SLOT = 16384;
constexpr int LDS_BYTES = 163840;
constexpr float SM_C = 0.08838834764831845f * 1.4426950408889634f;
constexpr float SM_HI = 30.f, SM_LO = 30.f;
constexpr float SM_THR = 8.f;

constexpr size_t WS_SY = 0;
constexpr size_t WS_O = 262144;
constexpr size_t WS_O_BYTES = (size_t)2 * NB * 4 * 256 * 1024;
constexpr size_t WS_P = WS_O + WS_O_BYTES;

DI unsigned rfl(unsigned x) { return __builtin_amdgcn_readfirstlane(x); }
DI unsigned cvtpk(float lo, float hi) { f32x2 v = {lo, hi}; bf16x2 b = __builtin_convertvector(v, bf16x2); return __builtin_bit_cast(unsigned, b); }
DI bf16x8 pack8(const f32x16& x, int base) {
  u32x4 w = {cvtpk(x[base + 0], x[base + 1]), cvtpk(x[base + 2], x[base + 3]), cvtpk(x[base + 4], x[base + 5]), cvtpk(x[base + 6], x[base + 7])};
  return __builtin_bit_cast(bf16x8, w);
}
DI s16x4 trread(unsigned a) { return __builtin_amdgcn_ds_read_tr16_b64_v4i16((LAS s16x4*)(size_t)a); }
DI bf16x8 cat(s16x4 lo, s16x4 hi) { s16x8 r = __builtin_shufflevector(lo, hi, 0, 1, 2, 3, 4, 5, 6, 7); return __builtin_bit_cast(bf16x8, r); }
DI bf16x8 ldsfrag(unsigned a) { return *(const LAS bf16x8*)(size_t)a; }
DI float swap32_add(float x) { auto r = __builtin_amdgcn_permlane32_swap(__float_as_uint(x), __float_as_uint(x), false, false); return __uint_as_float(r[0]) + __uint_as_float(r[1]); }
DI float max3f(float a, float b, float c) { return __builtin_fmaxf(__builtin_fmaxf(a, b), c); }

typedef __amdgpu_buffer_rsrc_t rsrc_t;
DI rsrc_t mkrsrc(const void* p, unsigned bytes) { return __builtin_amdgcn_make_buffer_rsrc((void*)p, (short)0, (int)bytes, 0x00020000); }

#define KVPOL "nt"
DI void dma4(rsrc_t r, unsigned voff, unsigned s0, unsigned s1, unsigned s2, unsigned s3, unsigned m0v) {
  unsigned keep;
  asm volatile("s_mov_b32 %0, m0\n\ts_mov_b32 m0, %7\n\ts_nop 0\n\t"
               "buffer_load_dwordx4 %1, %2, %3 offen lds\n\t"
               "buffer_load_dwordx4 %1, %2, %4 offen offset:1024 lds\n\t"
               "buffer_load_dwordx4 %1, %2, %5 offen offset:2048 lds\n\t"
               "buffer_load_dwordx4 %1, %2, %6 offen offset:3072 lds\n\t"
               "s_mov_b32 m0, %0"
               : "=&s"(keep) : "v"(voff), "s"(r), "s"(s0), "s"(s1), "s"(s2), "s"(s3), "s"(m0v) : "memory");
}
DI void dma3(rsrc_t r, unsigned voff, unsigned s0, unsigned s1, unsigned s2, unsigned m0v) {
  unsigned keep;
  asm volatile("s_mov_b32 %0, m0\n\ts_mov_b32 m0, %6\n\ts_nop 0\n\t"
               "buffer_load_dwordx4 %1, %2, %3 offen lds\n\t"
               "buffer_load_dwordx4 %1, %2, %4 offen offset:1024 lds\n\t"
               "buffer_load_dwordx4 %1, %2, %5 offen offset:2048 lds\n\t"
               "s_mov_b32 m0, %0"
               : "=&s"(keep) : "v"(voff), "s"(r), "s"(s0), "s"(s1), "s"(s2), "s"(m0v) : "memory");
}
DI void dma4_kv(rsrc_t r, unsigned v0, unsigned v1, unsigned v2, unsigned v3, unsigned soff, unsigned m0v) {
  unsigned keep;
  asm volatile("s_mov_b32 %0, m0\n\ts_mov_b32 m0, %7\n\ts_nop 0\n\t"
               "buffer_load_dwordx4 %1, %5, %6 offen " KVPOL " lds\n\t"
               "buffer_load_dwordx4 %2, %5, %6 offen offset:1024 " KVPOL " lds\n\t"
               "buffer_load_dwordx4 %3, %5, %6 offen offset:2048 " KVPOL " lds\n\t"
               "buffer_load_dwordx4 %4, %5, %6 offen offset:3072 " KVPOL " lds\n\t"
               "s_mov_b32 m0, %0"
               : "=&s"(keep) : "v"(v0), "v"(v1), "v"(v2), "v"(v3), "s"(r), "s"(soff), "s"(m0v) : "memory");
}
template <int N> DI void wait_bar() { asm volatile("s_waitcnt vmcnt(%0) lgkmcnt(0)\n\ts_barrier" :: "i"(N) : "memory"); }
template <int N> DI void wait_vm() { asm volatile("s_waitcnt vmcnt(%0)" :: "i"(N) : "memory"); }
#define SBAR() __builtin_amdgcn_sched_barrier(0)
#define BAR() asm volatile("s_barrier" ::: "memory")
#define LBAR() asm volatile("s_waitcnt lgkmcnt(0)\n\ts_barrier" ::: "memory")
#define PIN(x) asm volatile("" : "+v"(x))

#define STAMP_DECL
#define STAMP(i)
#define STAMPRT(i)
#define STAMP_OUT(k)
#define STAMP3(i)
#define STAMP_OUTL()
#define STAMP2(i)
#define PIN2(x)
#define ROTQ 1
#define ROTK 1
#define KV_AT 0

constexpr int NTHREADS = 768;
constexpr unsigned PBUF = 32768;
constexpr unsigned LDS_R0 = 6 * SLOT, LDS_R1 = 8 * SLOT, LDS_X = 4 * SLOT;

DI void dma2(rsrc_t r, unsigned v0, unsigned v1, unsigned s0, unsigned s1, unsigned m0v) {
  unsigned keep;
  asm volatile("s_mov_b32 %0, m0\n\ts_mov_b32 m0, %6\n\ts_nop 0\n\t"
               "buffer_load_dwordx4 %1, %3, %4 offen lds\n\t"
               "buffer_load_dwordx4 %2, %3, %5 offen offset:1024 lds\n\t"
               "s_mov_b32 m0, %0"
               : "=&s"(keep) : "v"(v0), "v"(v1), "s"(r), "s"(s0), "s"(s1), "s"(m0v) : "memory");
}
#define CODE_KB 0
#define WARMT 1
#define ZPAD 1
#define WARMO 1
#define HPAR 1
#define WARMW (!HPAR)
#define GPR 0
#define GPRIO(x) do { if (GPR) __builtin_amdgcn_s_setprio(x); } while (0)
#define PKS 1
#define PKF 1
#define KVREG 0
#define KVL 3
#define KV_A 0
typedef __attribute__((address_space(1))) unsigned gu32;
#define RLX_AGENT __ATOMIC_RELAXED, __HIP_MEMORY_SCOPE_AGENT
#define VM0() asm volatile("s_waitcnt vmcnt(0)" ::: "memory")
DI unsigned wo_slot(unsigned e) { return (unsigned)((0x3982103254769876ull >> (4 * e)) & 15) * SLOT; }

__global__ __launch_bounds__(NTHREADS, 3) void fused_kernel(const u16* __restrict__ xA, const u16* __restrict__ kA, const u16* __restrict__ vA, const u16* __restrict__ wqA, const u16* __restrict__ woA,
                                                            const u16* __restrict__ xB, const u16* __restrict__ kB, const u16* __restrict__ vB, const u16* __restrict__ wqB, const u16* __restrict__ woB,
                                                            char* __restrict__ ws, u16* __restrict__ out) {
  extern __shared__ __attribute__((aligned(16))) char lds[];
  const unsigned tid = threadIdx.x, lane = tid & 63;
  const unsigned w = rfl(tid >> 6);
  const unsigned xcd = blockIdx.x & 7, idx = blockIdx.x >> 3;
  const unsigned br = xcd >> 2, oct = xcd & 3, hpl = idx & 3, hp = oct * 4 + hpl, b = idx >> 2;
  const unsigned ldsb = (unsigned)(size_t)lds;
  const unsigned l4 = lane >> 4, l15 = lane & 15;
  gu32* const sy = (gu32*)(ws + WS_SY);
  const unsigned par = HPAR ? b & 1 : 0;
  const unsigned rotq = ROTQ ? (HPAR ? 2 * ((b >> 1) + hpl) : b + 2 * hpl) : 0;

  if (__builtin_expect(w >= 10, 1)) {
    const bool isx = w == 11;
    const rsrc_t rO = mkrsrc((br ? woB : woA) + (size_t)(oct * 1024) * DM + hpl * 128, (1024 * DM - hpl * 128) * 2);
    const unsigned vO = l4 * 1024 + 16 * (l15 ^ (l4 << 2));
    unsigned nonce = 0;
    {
      const rsrc_t r = mkrsrc((br ? xB : xA) + (size_t)b * SQ * DM, SQ * DM * 2);
      const unsigned v0 = (lane >> 3) * 1024 + 16 * ((lane & 7) ^ l4), v1 = (lane >> 3) * 1024 + 16 * ((lane & 7) ^ (4 + l4));
      auto issue = [&](unsigned u, unsigned buf) {
        const unsigned s0 = ((u + rotq) & 7) * 128 + (w - 10) * 65536, m0 = buf + 16384 + (w - 10) * 8192;
        dma2(r, v0, v1, s0, s0 + 7168, m0); dma2(r, v0, v1, s0 + 16384, s0 + 16384 + 7168, m0 + 2048); dma2(r, v0, v1, s0 + 32768, s0 + 32768 + 7168, m0 + 4096);
        if (!isx) dma2(r, v0, v1, s0 + 49152, s0 + 49152 + 7168, m0 + 6144);
      };
      unsigned old;
      asm volatile("global_load_dword %0, %1, %2 sc1" : "=v"(old) : "v"((lane < 12 ? lane : 0) * 4), "s"(sy + blockIdx.x * 128) : "memory");
      issue(0, ldsb + 2 * SLOT); issue(1, ldsb + 4 * SLOT);
#pragma nounroll
      for (unsigned i = 2; i < 8; ++i) { STAMP3(3 * i); if (isx) wait_vm<6>(); else wait_vm<8>(); STAMP3(3 * i + 1); BAR(); STAMP3(3 * i + 2); issue(i, ldsb + (2 + 2 * (i & 3)) * SLOT); }
      PIN(old);
      if (isx) {
        nonce = rfl(old) + 1;
        while (__builtin_amdgcn_ballot_w64(old == nonce) != 0) ++nonce;
        if (lane == 0) __hip_atomic_store(sy + blockIdx.x * 128 + 64, nonce, RLX_AGENT);
      }
      if (isx) wait_bar<7>(); else wait_bar<8>();
      wait_bar<0>();
      BAR(); issue(0, ldsb + LDS_R0);
      BAR(); issue(1, ldsb + LDS_R1);
      if (isx) wait_bar<6>(); else wait_bar<8>();
      u32x4 junk[3] = {};
      unsigned junkt = 0;
      {
        const char* pa = ws + WS_O + ((((size_t)br * NB + b) * 4 + (lane & 3)) * 256 + oct * 64 + hpl * 16) * 1024;
        if (lane == 4) pa = ws + WS_P + (((size_t)br * NB + b) * 4 + hpl) * 262144 + ((((oct + 1) & 3) * 4 + oct) * 4) * 4096;
        if (lane == 5) pa = (const char*)(out + (size_t)b * SQ * 1024 + br * 512 + hpl * 128 + oct * 32);
        asm volatile("global_load_dword %0, %1, off" : "+v"(junkt) : "v"(pa) : "memory");
      }
#pragma nounroll
      for (unsigned j = 2; j < 8; ++j) {
        BAR(); issue(j, ldsb + LDS_R0 + (j & 1) * PBUF);
        {
          const unsigned s0 = (128 * b + (w - 10) * 64 + (j < 7 ? (j - 2) * 12 : 52)) * 1024;
          asm volatile("buffer_load_dwordx4 %0, %3, %4, %5 offen\n\tbuffer_load_dwordx4 %1, %3, %4, %6 offen\n\tbuffer_load_dwordx4 %2, %3, %4, %7 offen"
                       : "+v"(junk[0]), "+v"(junk[1]), "+v"(junk[2]) : "v"(vO), "s"(rO), "s"(s0), "s"(s0 + 4096), "s"(s0 + 8192) : "memory");
        }
        if (isx) wait_bar<6 + 3 * WARMO>(); else wait_bar<8 + 3 * WARMO>();
      }
      BAR(); wait_bar<0>();
      asm volatile("" :: "v"(junk[0]), "v"(junk[1]), "v"(junk[2]));
      asm volatile("" :: "v"(junkt));
    }
    auto issueO = [&](unsigned e) {
      const unsigned t = e >> 1, hl = 2 * ((hpl + t) & 3) + ((t >> 2) ^ par);
      const unsigned s0 = (hl * 128 + (e & 1) * 64 + (w - 10) * 32) * 1024, m0 = ldsb + wo_slot(e) + (w - 10) * 8192;
      dma4(rO, vO, s0, s0 + 3072, s0 + 6144, s0 + 9216, m0);
      dma4(rO, vO, s0 + 16384, s0 + 16384 + 3072, s0 + 16384 + 6144, s0 + 16384 + 9216, m0 + 4096);
    };
    const unsigned mbR = xcd + 8 * (4 * b + (lane & 3)), mbC = 4 * br + (lane & 3) + 8 * idx;
    unsigned echo = 0;
    auto refresh = [&](unsigned stage, bool wait) {
      const unsigned me = stage < 2 ? hpl : oct, mb = stage < 2 ? mbR : mbC;
#pragma nounroll
      for (unsigned spin = 0; spin < (1u << 16); ++spin) {
        const unsigned ack = __hip_atomic_load(sy + blockIdx.x * 128 + 4 * stage + (lane & 3), RLX_AGENT);
        const unsigned e2 = __hip_atomic_load(sy + mb * 128 + 64, RLX_AGENT);
        if (lane < 4 && lane != me && e2 != echo) __hip_atomic_store(sy + mb * 128 + 4 * stage + me, e2, RLX_AGENT);
        echo = e2;
        if (!wait || __builtin_amdgcn_ballot_w64(ack != nonce && (lane & 3) != me) == 0) break;
        __builtin_amdgcn_s_sleep(1);
      }
      __builtin_amdgcn_fence(__ATOMIC_ACQUIRE, "wavefront");
    };
    unsigned echoC = 0, ack0 = 0;
    BAR(); issueO(0); issueO(1); issueO(2); issueO(3);
    BAR();
    if (isx) { echo = __hip_atomic_load(sy + mbR * 128 + 64, RLX_AGENT); echoC = __hip_atomic_load(sy + mbC * 128 + 64, RLX_AGENT); }
    BAR();
    wait_bar<16>(); BAR();
    BAR();
    BAR(); issueO(4); issueO(5);
    BAR(); BAR(); BAR();
    BAR();
    if (isx) { if (lane < 4 && lane != hpl) __hip_atomic_store(sy + mbR * 128 + hpl, echo, RLX_AGENT); }
    BAR(); BAR();
    BAR(); issueO(8);
    BAR(); issueO(9);
    if (isx) ack0 = __hip_atomic_load(sy + blockIdx.x * 128 + (lane & 3), RLX_AGENT);
    BAR();
    BAR();
    if (isx) { if (__builtin_amdgcn_ballot_w64(ack0 != nonce && (lane & 3) != hpl) != 0) refresh(0, true); }
    issueO(6); issueO(7);
    wait_bar<16>(); issueO(10);
    if (isx) refresh(0, false);
    wait_bar<8>();
    if (isx) { if (lane < 4 && lane != hpl) __hip_atomic_store(sy + mbR * 128 + 4 + hpl, echo, RLX_AGENT); }
    issueO(13); issueO(11); issueO(14); issueO(12); issueO(15);
    if (isx) refresh(1, true);
    wait_bar<0>();
    if (!isx) return;
    BAR();
    BAR();
    echo = echoC;
    if (lane < 4 && lane != oct) __hip_atomic_store(sy + mbC * 128 + 8 + oct, echo, RLX_AGENT);
    refresh(2, true);
    BAR();
    refresh(2, false);
    return;
  }
  if (w >= 8) {
    const unsigned kbase = l4 * 256 + 16 * (l15 ^ l4);
    const unsigned vV = l4 * 256 + 16 * (l15 ^ (l4 << 2));
    const size_t kvoff = ((size_t)b * NH + 2 * hp) * SK * DH;
    const rsrc_t rK = mkrsrc((br ? kB : kA) + kvoff, 2 * SK * DH * 2);
    const rsrc_t rV = mkrsrc((br ? vB : vA) + kvoff, 2 * SK * DH * 2);
#define KVMASK 7
    const unsigned rotk = ROTK ? b + hp : 0, half = (w - 8) * 8192;
    unsigned slot = ldsb + half;
    auto issue = [&](unsigned n) {
      const unsigned so = ((n >> 4) ^ par) * 8 * SLOT + (((n >> 1) + rotk) & KVMASK) * SLOT + half;
      if (n & 1) { dma4_kv(rV, vV, vV, vV, vV, so, slot); dma4_kv(rV, vV, vV, vV, vV, so + 4096, slot + 4096); }
      else { dma4_kv(rK, kbase, kbase ^ 64, kbase ^ 128, kbase ^ 192, so, slot); dma4_kv(rK, kbase, kbase ^ 64, kbase ^ 128, kbase ^ 192, so + 4096, slot + 4096); }
      slot += SLOT; if (slot >= ldsb + 6 * SLOT) slot = ldsb + half;
    };
    if (KV_A < -1) { issue(0); issue(1); }
    BAR();
#pragma nounroll
    for (int i = 0; i < 8; ++i) {
      BAR();
      if (i == KV_A) { issue(0); issue(1); }
      if (i == 5) { if (KVL > 2) issue(2); }
    }
    static_assert(KVL == 2 || KVL == 3, "ring depth");
    wait_bar<8 * (KVL - 1)>(); issue(KVL);
#pragma nounroll
    for (unsigned n = 1; n < 29; n += 2) { BAR(); issue(n + KVL); wait_bar<8 * (KVL - 1)>(); issue(n + 1 + KVL); }
    BAR(); if (KVL == 2) issue(31);
    wait_bar<8>(); BAR();
    wait_bar<0>();
    return;
  }

  const unsigned r32 = lane & 31, hi = lane >> 5, g = w >> 2, wq = w & 3;
  STAMP_DECL; STAMPRT(62); STAMP(0);
  const unsigned tq = l15 >> 2, tp = lane & 3, blk = l4 & 1;
  const unsigned tr0 = 256 * tq + 64 * tq + 32 * blk + 16 * (tp & 1) + 8 * (tp >> 1);
  const unsigned xrow = (32 * wq + r32 < SQ) ? 32 * wq + r32 : 32 * wq + r32 - 16;
  const unsigned xa0 = 16384 + 128 * xrow + 16 * (hi ^ ((xrow >> 1) & 7));
  const unsigned ka0 = 256 * r32 + 16 * (hi ^ (r32 & 15));

  f32x16 acc[4];
#pragma unroll
  for (int c = 0; c < 4; ++c) acc[c] = f32x16{};
  const rsrc_t rW = mkrsrc((br ? wqB : wqA) + (size_t)hp * 256, (DM * HD - hp * 256) * 2);
  auto issueW = [&](unsigned gg, unsigned u, unsigned buf) {
    const unsigned s0 = (gg ^ par) * 256 + (64 * ((u + rotq) & 7) + 16 * wq) * 8192;
    dma4(rW, l4 * 8192 + 16 * (l15 ^ (l4 << 2)), s0, s0 + 31744, s0 + 2 * 31744, s0 + 3 * 31744, buf + wq * 4096);
  };
#define QFIRST(wb, xb) do { xf[0] = ldsfrag(xb); _Pragma("unroll") for (int i = 0; i < 4; ++i) { lo[i] = trread((wb) ^ (64 * i)); hi_[i] = trread(((wb) ^ (64 * i)) + 1024); } } while (0)
#define QGAP(i) do { acc[(i) & 3] = MFMA(cat(lo[i], hi_[i]), xf[(i) >> 2], acc[(i) & 3]); \
    if ((i) + 4 < 16) { if (((i) & 3) == 0) xf[((i) >> 2) + 1] = ldsfrag(xb ^ (32 * (((i) >> 2) + 1))); \
      lo[(i) + 4] = trread((wb ^ (64 * ((i) & 3))) + 4096 * (((i) + 4) >> 2)); hi_[(i) + 4] = trread((wb ^ (64 * ((i) & 3))) + 4096 * (((i) + 4) >> 2) + 1024); } } while (0)
  if (g) {
    issueW(0, 0, ldsb + 2 * SLOT); issueW(0, 1, ldsb + 4 * SLOT);
#pragma nounroll
    for (unsigned i = 2; i < 8; ++i) { wait_bar<4>(); issueW(0, i, ldsb + (2 + 2 * (i & 3)) * SLOT); }
    wait_bar<4>(); wait_bar<0>();
    BAR(); issueW(1, 0, ldsb + LDS_R0);
    BAR(); issueW(1, 1, ldsb + LDS_R1);
#pragma nounroll
    for (unsigned u = 0; u < 8; ++u) {
      const unsigned buf = ldsb + LDS_R0 + (u & 1) * PBUF, wb = buf + hi * 2048 + tr0, xb = buf + xa0;
      bf16x8 xf[4]; s16x4 lo[16], hi_[16];
      if (u < 7) wait_bar<4>(); else wait_bar<0>();
      STAMP(1 + u);
#pragma unroll
      for (int i = 0; i < 16; ++i) {
        if ((i & 3) == 0) xf[i >> 2] = ldsfrag(xb ^ (32 * (i >> 2)));
        lo[i] = trread((wb ^ (64 * (i & 3))) + 4096 * (i >> 2)); hi_[i] = trread((wb ^ (64 * (i & 3))) + 4096 * (i >> 2) + 1024);
      }
      SBAR();
#pragma unroll
      for (int i = 0; i < 8; ++i) { acc[i & 3] = MFMA(cat(lo[i], hi_[i]), xf[i >> 2], acc[i & 3]); SBAR(); }
      if (u < 7) LBAR();
      if (u < 6) issueW(1, u + 2, buf);
      SBAR();
#pragma unroll
      for (int i = 8; i < 16; ++i) { acc[i & 3] = MFMA(cat(lo[i], hi_[i]), xf[i >> 2], acc[i & 3]); SBAR(); }
    }
    BAR();
  } else {
    bf16x8 xf[4]; s16x4 lo[16], hi_[16];
    unsigned wb = ldsb + 2 * SLOT + hi * 2048 + tr0, xb = ldsb + 2 * SLOT + xa0;
    BAR();
    QFIRST(wb, xb);
#pragma nounroll
    for (unsigned u = 0; u < 8; ++u) {
      STAMP(1 + u);
      const unsigned d = (u & 3) == 3 ? -3 * PBUF : PBUF, wn = wb + d, xn = xb + d;
      SBAR();
#pragma unroll
      for (int i = 0; i < 16; ++i) {
        QGAP(i);
        if (i >= 12) { if (i == 12) xf[0] = ldsfrag(xn); lo[i - 12] = trread(wn ^ (64 * (i & 3))); hi_[i - 12] = trread((wn ^ (64 * (i & 3))) + 1024); }
        SBAR();
        if (i == 11) { BAR(); SBAR(); }
      }
      wb = wn; xb = xn;
    }
  }
  bf16x8 qf[8];
#pragma unroll
  for (int c = 0; c < 4; ++c) { acc[c] *= SM_C; qf[2 * c] = pack8(acc[c], 0); qf[2 * c + 1] = pack8(acc[c], 8); }
  if (32 * wq + r32 >= SQ) {
#pragma unroll
    for (int c = 0; c < 8; ++c) qf[c] = bf16x8{};
  }

#pragma unroll
  for (int c = 0; c < 4; ++c) acc[c] = f32x16{};
  float m = 0.f, l = 0.f, hi_thr = SM_HI;
  unsigned ksl = ldsb + (g ? 4 * SLOT : 0), vsl = ksl;
  STAMP(9);
  float l2 = 0.f;
  f32x16 s0, s1; unsigned pA[8], pB[8] = {0, 0, 0, 0, 0, 0, 0, 0};
#define PFR(P, k) __builtin_bit_cast(bf16x8, u32x4{P[4 * (k)], P[4 * (k) + 1], P[4 * (k) + 2], P[4 * (k) + 3]})
  s16x4 vlo[8], vhi[8]; bf16x8 kf[8];
#define NEXTSLOT(x) ((x) + SLOT == ldsb + 6 * SLOT ? ldsb : (x) + SLOT)
#define VRD(i, vb, vo) do { vlo[i] = trread(((vb) ^ (64 * ((i) & 3))) + (vo) + 4096 * ((i) >> 2)); vhi[i] = trread(((vb) ^ (64 * ((i) & 3))) + (vo) + 4096 * ((i) >> 2) + 2048); } while (0)
#define ELO(i) ((i) < 4 ? 0 : (16 * ((i) - 4)) / 12)
#define HSTEP(SC, SN, PI, PO, vb, vo, kb, ko, vn, vno) do { \
    float mx0, mx1, alpha = 1.f, pe[16]; bool pend = false; \
    SBAR(); \
    _Pragma("unroll") for (int i = 0; i < 16; ++i) { \
      if (i < 8) acc[i & 3] = MFMA(cat(vlo[i], vhi[i]), PFR(PI, i >> 2), acc[i & 3]); \
      else if (i == 8) SN = MFMA(kf[0], qf[0], f32x16{}); else SN = MFMA(kf[i - 8], qf[i - 8], SN); \
      if (i < 5) VRD(i + 3, vb, vo); else if (i < 13) kf[i - 5] = ldsfrag(((kb) ^ (32 * (i - 5))) + (ko)); else VRD(i - 13, vn, vno); \
      if (i == 1) { mx0 = max3f(SC[0], SC[1], SC[2]); mx1 = max3f(SC[8], SC[9], SC[10]); mx0 = max3f(mx0, SC[3], SC[4]); mx1 = max3f(mx1, SC[11], SC[12]); PIN(mx0); PIN(mx1); } \
      if (i == 2) { mx0 = max3f(SC[5], SC[6], mx0); mx1 = max3f(SC[13], SC[14], mx1); mx0 = max3f(SC[7], SC[15], mx0); mx0 = __builtin_fmaxf(mx0, mx1); PIN(mx0); } \
      if (i == 3) {     \
        if (__builtin_expect(__builtin_amdgcn_ballot_w64(!(mx0 < hi_thr)) != 0, 0)) { \
          hi_thr = -__builtin_inff(); \
          if (__builtin_amdgcn_ballot_w64(!(mx0 - m <= SM_THR)) != 0) {     \
            auto sw = __builtin_amdgcn_permlane32_swap(__float_as_uint(mx0), __float_as_uint(mx0), false, false); \
            const float mn = max3f(m, __uint_as_float(sw[0]), __uint_as_float(sw[1])); alpha = __builtin_amdgcn_exp2f(m - mn); m = mn; l *= alpha; l2 *= alpha; pend = true; } \
          _Pragma("unroll") for (int e = 0; e < 16; ++e) SC[e] -= m; } } \
      if (i >= 5) { _Pragma("unroll") for (int e = ELO(i - 1); e < ELO(i); ++e) { if (e & 1) { l2 += pe[e]; PO[e >> 1] = cvtpk(pe[e - 1], pe[e]); PIN(PO[e >> 1]); } else l += pe[e]; } PIN(l); PIN(l2); } \
      if (i >= 4) { _Pragma("unroll") for (int e = ELO(i); e < ELO(i + 1); ++e) { pe[e] = __builtin_amdgcn_exp2f(SC[e]); PIN(pe[e]); } } \
      SBAR(); \
      if (i == 7) { if (__builtin_expect(pend, 0)) { _Pragma("unroll") for (int c = 0; c < 4; ++c) acc[c] *= alpha; } SBAR(); } \
      if (i == 12) { BAR(); SBAR(); } \
    } \
    _Pragma("unroll") for (int e = ELO(15); e < 16; ++e) { if (e & 1) { l2 += pe[e]; PO[e >> 1] = cvtpk(pe[e - 1], pe[e]); } else l += pe[e]; } \
    PIN(PO[7]); PIN(l); PIN(l2); \
  } while (0)
  if (!g) LBAR();
  {
    const unsigned kb = ksl + ka0, vb = vsl + hi * 1024 + tr0;
#pragma unroll
    for (int i = 0; i < 8; ++i) kf[i] = ldsfrag(kb ^ (32 * i));
#pragma unroll
    for (int i = 0; i < 3; ++i) VRD(i, vb, 8192);
    s0 = MFMA(kf[0], qf[0], f32x16{});
#pragma unroll
    for (int i = 1; i < 8; ++i) s0 = MFMA(kf[i], qf[i], s0);
    {
      float mx = max3f(s0[0], s0[1], s0[2]);
#pragma unroll
      for (int e = 3; e < 15; e += 2) mx = max3f(mx, s0[e], s0[e + 1]);
      mx = __builtin_fmaxf(mx, s0[15]);
      auto sw = __builtin_amdgcn_permlane32_swap(__float_as_uint(mx), __float_as_uint(mx), false, false);
      mx = __builtin_fmaxf(__uint_as_float(sw[0]), __uint_as_float(sw[1]));
      if (__builtin_amdgcn_ballot_w64(!(mx >= -SM_LO)) != 0) { hi_thr = -__builtin_inff(); m = mx; }
    }
    SBAR();
    BAR();
  }
#pragma nounroll
  for (unsigned j = 0; j < 8; ++j) {
    STAMP(10 + 4 * j);
    const unsigned vnext = NEXTSLOT(ksl), knext = NEXTSLOT(vnext);
    const unsigned vb0 = vsl + hi * 1024 + tr0, kb0 = ksl + ka0, vb1 = vnext + hi * 1024 + tr0, kb1 = knext + ka0;
    HSTEP(s0, s1, pB, pA, vb0, 8192, kb0, 8192, vb1, 0);
    HSTEP(s1, s0, pA, pB, vb1, 0, kb1, 0, vb1, 8192);
    vsl = vnext; ksl = knext;
  }
  {
    const unsigned vb = vsl + hi * 1024 + tr0;
    SBAR();
#pragma unroll
    for (int i = 0; i < 8; ++i) {
      acc[i & 3] = MFMA(cat(vlo[i], vhi[i]), PFR(pB, i >> 2), acc[i & 3]);
      if (i < 5) VRD(i + 3, vb, 8192);
      SBAR();
    }
  }
  STAMP(46);
  unsigned laneT; asm volatile("v_mbcnt_lo_u32_b32 %0, -1, 0\n\tv_mbcnt_hi_u32_b32 %0, -1, %0" : "=v"(laneT));
  const unsigned r32T = laneT & 31, hiT = laneT >> 5, tqT = (laneT & 15) >> 2, tpT = laneT & 3, blkT = (laneT >> 4) & 1;
  const unsigned vo16 = (32 * wq + r32T < SQ) ? laneT * 16 : 0x80000000u;
  const rsrc_t rOf = mkrsrc(ws + WS_O + ((((size_t)br * NB + b) * 4 + wq) * 256 + oct * 64) * 1024, 65536);
  bf16x8 of[8];
  {
    const float inv = 1.f / swap32_add(l + l2);
    __builtin_amdgcn_s_setprio(3);
#pragma unroll
    for (int c = 0; c < 4; ++c) {
      acc[c] *= inv; of[2 * c] = pack8(acc[c], 0); of[2 * c + 1] = pack8(acc[c], 8);
      __builtin_amdgcn_raw_buffer_store_b128(__builtin_bit_cast(u32x4, of[2 * c]), rOf, vo16, (2 * hpl + (g ^ par)) * 8192 + 2 * c * 1024, 16);
      __builtin_amdgcn_raw_buffer_store_b128(__builtin_bit_cast(u32x4, of[2 * c + 1]), rOf, vo16, (2 * hpl + (g ^ par)) * 8192 + (2 * c + 1) * 1024, 16);
      if (c == 1 && !g) BAR();
    }
    __builtin_amdgcn_s_setprio(0);
  }
  STAMP(47);
#pragma unroll
  for (int c = 0; c < 4; ++c) acc[c] = f32x16{};
  const unsigned tro = ldsb + hiT * 2048 + 256 * tqT + 64 * tqT + 32 * (tpT & 1) + 16 * blkT + 8 * (tpT >> 1);
#define NBAR(n) do { _Pragma("nounroll") for (unsigned i_ = (n); i_ > 0; --i_) LBAR(); } while (0)
#define OITEM(F, fo, e) do { const unsigned wb = wo_slot(e) + tro; s16x4 lo[16], hi_[16]; SBAR(); \
    _Pragma("unroll") for (int i = 0; i < 4; ++i) { lo[i] = trread(wb ^ (64 * i)); hi_[i] = trread((wb ^ (64 * i)) + 1024); } \
    SBAR(); \
    _Pragma("unroll") for (int i = 0; i < 16; ++i) {     \
      acc[i & 3] = MFMA(cat(lo[i], hi_[i]), F[(fo) + (i >> 2)], acc[i & 3]); \
      if (i + 4 < 16) { lo[i + 4] = trread((wb ^ (64 * (i & 3))) + 4096 * ((i + 4) >> 2)); hi_[i + 4] = trread((wb ^ (64 * (i & 3))) + 4096 * ((i + 4) >> 2) + 1024); } \
      SBAR(); } } while (0)
  NBAR(g ? 0 : 1); OITEM(of, 0, 8 * g);
  NBAR(g ? 0 : 2); OITEM(of, 4, 8 * g + 1);
  STAMP(49);
  NBAR(g ? 0 : 4);
  wait_bar<0>();
#pragma nounroll
  for (unsigned r = g ? 2 : 0; r < 3; ++r) {
    NBAR(r == 0 ? 7 : r == 1 ? 0 : 1);
    const unsigned e0 = r == 2 ? 10 + 3 * g : 2 + 3 * r;
    bf16x8 pf[12];
#pragma unroll
    for (int k = 0; k < 3; ++k) {
      const unsigned e = e0 + k, t = e >> 1, so = (2 * ((hpl + t) & 3) + ((t >> 2) ^ par)) * 8192 + (e & 1) * 4096;
#pragma unroll
      for (int s = 0; s < 4; ++s) pf[4 * k + s] = __builtin_bit_cast(bf16x8, __builtin_amdgcn_raw_buffer_load_b128(rOf, vo16, so + s * 1024, 16));
    }
    STAMP(50 + 2 * r);
    NBAR(r == 1);
    OITEM(pf, 0, e0); OITEM(pf, 4, e0 + 1); OITEM(pf, 8, e0 + 2);
    STAMP(51 + 2 * r);
  }
  {
    LAS f32x4* xw = (LAS f32x4*)(size_t)(ldsb + 4 * SLOT + w * 8192 + laneT * 16);
    const LAS f32x4* xr = (const LAS f32x4*)(size_t)(ldsb + 4 * SLOT + (w ^ 4) * 8192 + laneT * 16);
#define XSEND(A, c) _Pragma("unroll") for (int q4 = 0; q4 < 4; ++q4) xw[((c) * 4 + q4) * 64] = f32x4{A[4 * q4], A[4 * q4 + 1], A[4 * q4 + 2], A[4 * q4 + 3]}
    if (g) { XSEND(acc[0], 0); XSEND(acc[1], 1); acc[0] = acc[2]; acc[1] = acc[3]; } else { XSEND(acc[2], 0); XSEND(acc[3], 1); }
    LBAR();
#pragma unroll
    for (int c = 0; c < 2; ++c)
#pragma unroll
      for (int q4 = 0; q4 < 4; ++q4) { const f32x4 o = xr[(c * 4 + q4) * 64];
#pragma unroll
        for (int k = 0; k < 4; ++k) acc[c][4 * q4 + k] += o[k]; }
  }
  STAMP(57);
  const rsrc_t rP = mkrsrc(ws + WS_P + (((size_t)br * NB + b) * 4 + hpl) * 262144, 262144);
#pragma unroll
  for (int c = 0; c < 2; ++c) {
    if (2 * g + c != oct) {
#pragma unroll
      for (int q4 = 0; q4 < 4; ++q4)
        __builtin_amdgcn_raw_buffer_store_b128(__builtin_bit_cast(u32x4, f32x4{acc[c][4 * q4], acc[c][4 * q4 + 1], acc[c][4 * q4 + 2], acc[c][4 * q4 + 3]}), rP, vo16, (((2 * g + c) * 4 + oct) * 4 + wq) * 4096 + q4 * 1024, 16);
    }
  }
  if (oct & 1) acc[0] = acc[1];
  VM0();
  STAMP(58);
  BAR();
  if (g != (oct >> 1)) return;
  BAR();
  STAMP(59);
  if (g == (oct >> 1)) {
    f32x4 pv[12];
#pragma unroll
    for (int k = 0; k < 3; ++k)
#pragma unroll
      for (int q4 = 0; q4 < 4; ++q4)
        pv[4 * k + q4] = __builtin_bit_cast(f32x4, __builtin_amdgcn_raw_buffer_load_b128(rP, vo16, ((oct * 4 + ((oct + 1 + k) & 3)) * 4 + wq) * 4096 + q4 * 1024, 16));
#pragma unroll
    for (int k = 0; k < 3; ++k)
#pragma unroll
      for (int r = 0; r < 16; ++r) acc[0][r] += pv[4 * k + (r >> 2)][r & 3];
    const unsigned q = 32 * wq + r32T;
    if (q < SQ) {
      u16* o = out + ((size_t)b * SQ + q) * 1024 + br * 512 + hpl * 128 + oct * 32 + hiT * 16;
      *(bf16x8*)o = pack8(acc[0], 0);
      *(bf16x8*)(o + 8) = pack8(acc[0], 8);
    }
  }
  STAMP(48); STAMPRT(63); STAMP_OUT(0);
}

extern "C" void kernel_launch(void* const* d_in, const int* in_sizes, int n_in, void* d_out, int out_size, void* d_ws, size_t ws_size, hipStream_t stream) {
  const u16* xA = (const u16*)d_in[0]; const u16* kA = (const u16*)d_in[1]; const u16* vA = (const u16*)d_in[2]; const u16* wqA = (const u16*)d_in[3]; const u16* woA = (const u16*)d_in[4];
  const u16* xB = (const u16*)d_in[5]; const u16* kB = (const u16*)d_in[6]; const u16* vB = (const u16*)d_in[7]; const u16* wqB = (const u16*)d_in[8]; const u16* woB = (const u16*)d_in[9];
  (void)hipFuncSetAttribute((const void*)fused_kernel, hipFuncAttributeMaxDynamicSharedMemorySize, LDS_BYTES);
  fused_kernel<<<256, NTHREADS, LDS_BYTES, stream>>>(xA, kA, vA, wqA, woA, xB, kB, vB, wqB, woB, (char*)d_ws, (u16*)d_out);
}
```

```cpp
#include <hip/hip_runtime.h>
#include <hip/hip_bf16.h>

typedef __bf16 bf16x8 __attribute__((ext_vector_type(8)));
typedef __bf16 bf16x2 __attribute__((ext_vector_type(2)));
typedef short s16x4 __attribute__((ext_vector_type(4)));
typedef short s16x8 __attribute__((ext_vector_type(8)));
typedef float f32x16 __attribute__((ext_vector_type(16)));
typedef float f32x4 __attribute__((ext_vector_type(4)));
typedef float f32x2 __attribute__((ext_vector_type(2)));
typedef unsigned u32x4 __attribute__((ext_vector_type(4)));
typedef unsigned u32x2 __attribute__((ext_vector_type(2)));
typedef unsigned short u16;
#define LAS __attribute__((address_space(3)))
#define DI __device__ __forceinline__
#define MFMA(a, b, c) __builtin_amdgcn_mfma_f32_32x32x16_bf16((a), (b), (c), 0, 0, 0)

constexpr int NB = 8, SQ = 112, DM = 512, NH = 32, SK = 512, DH = 128, HD = NH * DH;
constexpr unsigned SLOT = 16384;
constexpr int LDS_BYTES = 163840;
constexpr float SM_C = 0.08838834764831845f * 1.4426950408889634f;
constexpr float SM_HI = 30.f, SM_LO = 30.f;
constexpr float SM_THR = 8.f;

constexpr size_t WS_SY = 0;
constexpr size_t WS_O = 262144;
constexpr size_t WS_O_BYTES = (size_t)2 * NB * 4 * 256 * 1024;
constexpr size_t WS_P = WS_O + WS_O_BYTES;

DI unsigned rfl(unsigned x) { return __builtin_amdgcn_readfirstlane(x); }
DI unsigned cvtpk(float lo, float hi) { f32x2 v = {lo, hi}; bf16x2 b = __builtin_convertvector(v, bf16x2); return __builtin_bit_cast(unsigned, b); }
DI bf16x8 pack8(const f32x16& x, int base) {
  u32x4 w = {cvtpk(x[base + 0], x[base + 1]), cvtpk(x[base + 2], x[base + 3]), cvtpk(x[base + 4], x[base + 5]), cvtpk(x[base + 6], x[base + 7])};
  return __builtin_bit_cast(bf16x8, w);
}
DI s16x4 trread(unsigned a) { return __builtin_amdgcn_ds_read_tr16_b64_v4i16((LAS s16x4*)(size_t)a); }
DI bf16x8 cat(s16x4 lo, s16x4 hi) { s16x8 r = __builtin_shufflevector(lo, hi, 0, 1, 2, 3, 4, 5, 6, 7); return __builtin_bit_cast(bf16x8, r); }
DI bf16x8 ldsfrag(unsigned a) { return *(const LAS bf16x8*)(size_t)a; }
DI float swap32_add(float x) { auto r = __builtin_amdgcn_permlane32_swap(__float_as_uint(x), __float_as_uint(x), false, false); return __uint_as_float(r[0]) + __uint_as_float(r[1]); }
DI float max3f(float a, float b, float c) { return __builtin_fmaxf(__builtin_fmaxf(a, b), c); }
DI float max3v(float a, float b, float c) { float r; asm("v_max3_f32 %0, %1, %2, %3" : "=v"(r) : "v"(a), "v"(b), "v"(c)); return r; }

typedef __amdgpu_buffer_rsrc_t rsrc_t;
DI rsrc_t mkrsrc(const void* p, unsigned bytes) { return __builtin_amdgcn_make_buffer_rsrc((void*)p, (short)0, (int)bytes, 0x00020000); }

#define KVPOL "nt"
DI void dma4(rsrc_t r, unsigned voff, unsigned s0, unsigned s1, unsigned s2, unsigned s3, unsigned m0v) {
  unsigned keep;
  asm volatile("s_mov_b32 %0, m0\n\ts_mov_b32 m0, %7\n\ts_nop 0\n\t"
               "buffer_load_dwordx4 %1, %2, %3 offen lds\n\t"
               "buffer_load_dwordx4 %1, %2, %4 offen offset:1024 lds\n\t"
               "buffer_load_dwordx4 %1, %2, %5 offen offset:2048 lds\n\t"
               "buffer_load_dwordx4 %1, %2, %6 offen offset:3072 lds\n\t"
               "s_mov_b32 m0, %0"
               : "=&s"(keep) : "v"(voff), "s"(r), "s"(s0), "s"(s1), "s"(s2), "s"(s3), "s"(m0v) : "memory");
}
DI void dma3(rsrc_t r, unsigned voff, unsigned s0, unsigned s1, unsigned s2, unsigned m0v) {
  unsigned keep;
  asm volatile("s_mov_b32 %0, m0\n\ts_mov_b32 m0, %6\n\ts_nop 0\n\t"
               "buffer_load_dwordx4 %1, %2, %3 offen lds\n\t"
               "buffer_load_dwordx4 %1, %2, %4 offen offset:1024 lds\n\t"
               "buffer_load_dwordx4 %1, %2, %5 offen offset:2048 lds\n\t"
               "s_mov_b32 m0, %0"
               : "=&s"(keep) : "v"(voff), "s"(r), "s"(s0), "s"(s1), "s"(s2), "s"(m0v) : "memory");
}
DI void dma4_kv(rsrc_t r, unsigned v0, unsigned v1, unsigned v2, unsigned v3, unsigned soff, unsigned m0v) {
  unsigned keep;
  asm volatile("s_mov_b32 %0, m0\n\ts_mov_b32 m0, %7\n\ts_nop 0\n\t"
               "buffer_load_dwordx4 %1, %5, %6 offen " KVPOL " lds\n\t"
               "buffer_load_dwordx4 %2, %5, %6 offen offset:1024 " KVPOL " lds\n\t"
               "buffer_load_dwordx4 %3, %5, %6 offen offset:2048 " KVPOL " lds\n\t"
               "buffer_load_dwordx4 %4, %5, %6 offen offset:3072 " KVPOL " lds\n\t"
               "s_mov_b32 m0, %0"
               : "=&s"(keep) : "v"(v0), "v"(v1), "v"(v2), "v"(v3), "s"(r), "s"(soff), "s"(m0v) : "memory");
}
template <int N> DI void wait_bar() { asm volatile("s_waitcnt vmcnt(%0) lgkmcnt(0)\n\ts_barrier" :: "i"(N) : "memory"); }
template <int N> DI void wait_vm() { asm volatile("s_waitcnt vmcnt(%0)" :: "i"(N) : "memory"); }
#define SBAR() __builtin_amdgcn_sched_barrier(0)
#define BAR() asm volatile("s_barrier" ::: "memory")
#define LBAR() asm volatile("s_waitcnt lgkmcnt(0)\n\ts_barrier" ::: "memory")
#define PIN(x) asm volatile("" : "+v"(x))

#define STAMP_DECL
#define STAMP(i)
#define STAMPRT(i)
#define STAMP_OUT(k)
#define STAMP3(i)
#define STAMP_OUTL()
#define STAMP2(i)
#define PIN2(x)
#define ROTQ 1
#define ROTK 1
#define KV_AT 0

constexpr int NTHREADS = 768;
constexpr unsigned PBUF = 32768;
constexpr unsigned LDS_R0 = 6 * SLOT, LDS_R1 = 8 * SLOT, LDS_X = 4 * SLOT;

DI void dma2(rsrc_t r, unsigned v0, unsigned v1, unsigned s0, unsigned s1, unsigned m0v) {
  unsigned keep;
  asm volatile("s_mov_b32 %0, m0\n\ts_mov_b32 m0, %6\n\ts_nop 0\n\t"
               "buffer_load_dwordx4 %1, %3, %4 offen lds\n\t"
               "buffer_load_dwordx4 %2, %3, %5 offen offset:1024 lds\n\t"
               "s_mov_b32 m0, %0"
               : "=&s"(keep) : "v"(v0), "v"(v1), "s"(r), "s"(s0), "s"(s1), "s"(m0v) : "memory");
}
#define CODE_KB 20
#define WARMT 1
#define ZPAD 1
#define WARMO 1
#define HPAR 1
#define WARMW (!HPAR)
#define GPR 0
#define GPRIO(x) do { if (GPR) __builtin_amdgcn_s_setprio(x); } while (0)
#define PKS 1
#define PKF 1
#define KEEPV 1
#define OPOL 16
#define KVREG 0
#define KVL 3
#define KV_A 0
typedef __attribute__((address_space(1))) unsigned gu32;
#define RLX_AGENT __ATOMIC_RELAXED, __HIP_MEMORY_SCOPE_AGENT
#define VM0() asm volatile("s_waitcnt vmcnt(0)" ::: "memory")
DI unsigned wo_slot(unsigned e) { return (unsigned)((0x3982103254769876ull >> (4 * e)) & 15) * SLOT; }

__global__ __launch_bounds__(NTHREADS, 3) void fused_kernel(const u16* __restrict__ xA, const u16* __restrict__ kA, const u16* __restrict__ vA, const u16* __restrict__ wqA, const u16* __restrict__ woA,
                                                            const u16* __restrict__ xB, const u16* __restrict__ kB, const u16* __restrict__ vB, const u16* __restrict__ wqB, const u16* __restrict__ woB,
                                                            char* __restrict__ ws, u16* __restrict__ out) {
  extern __shared__ __attribute__((aligned(16))) char lds[];
  unsigned long long pc0; asm volatile("s_getpc_b64 %0" : "=s"(pc0));
  const unsigned tid = threadIdx.x, lane = tid & 63;
  const unsigned w = rfl(tid >> 6);
  const unsigned xcd = blockIdx.x & 7, idx = blockIdx.x >> 3;
  const unsigned br = xcd >> 2, oct = xcd & 3, hpl = idx & 3, hp = oct * 4 + hpl, b = idx >> 2;
  const unsigned ldsb = (unsigned)(size_t)lds;
  const unsigned l4 = lane >> 4, l15 = lane & 15;
  gu32* const sy = (gu32*)(ws + WS_SY);
  const unsigned par = HPAR ? b & 1 : 0;
  const unsigned rotq = ROTQ ? (HPAR ? 2 * ((b >> 1) + hpl) : b + 2 * hpl) : 0;

  if (__builtin_expect(w >= 10, 1)) {
    const bool isx = w == 11;
    const rsrc_t rO = mkrsrc((br ? woB : woA) + (size_t)(oct * 1024) * DM + hpl * 128, (1024 * DM - hpl * 128) * 2);
    const unsigned vO = l4 * 1024 + 16 * (l15 ^ (l4 << 2));
    unsigned nonce = 0;
    {
      const rsrc_t r = mkrsrc((br ? xB : xA) + (size_t)b * SQ * DM, SQ * DM * 2);
      const unsigned v0 = (lane >> 3) * 1024 + 16 * ((lane & 7) ^ l4), v1 = (lane >> 3) * 1024 + 16 * ((lane & 7) ^ (4 + l4));
      auto issue = [&](unsigned u, unsigned buf) {
        const unsigned s0 = ((u + rotq) & 7) * 128 + (w - 10) * 65536, m0 = buf + 16384 + (w - 10) * 8192;
        dma2(r, v0, v1, s0, s0 + 7168, m0); dma2(r, v0, v1, s0 + 16384, s0 + 16384 + 7168, m0 + 2048); dma2(r, v0, v1, s0 + 32768, s0 + 32768 + 7168, m0 + 4096);
        if (!isx) dma2(r, v0, v1, s0 + 49152, s0 + 49152 + 7168, m0 + 6144);
      };
      unsigned old;
      asm volatile("global_load_dword %0, %1, %2 sc1" : "=v"(old) : "v"((lane < 12 ? lane : 0) * 4), "s"(sy + blockIdx.x * 128) : "memory");
      issue(0, ldsb + 2 * SLOT); issue(1, ldsb + 4 * SLOT);
#pragma nounroll
      for (unsigned i = 2; i < 8; ++i) { STAMP3(3 * i); if (isx) wait_vm<6>(); else wait_vm<8>(); STAMP3(3 * i + 1); BAR(); STAMP3(3 * i + 2); issue(i, ldsb + (2 + 2 * (i & 3)) * SLOT); }
      PIN(old);
      if (isx) {
        nonce = rfl(old) + 1;
        while (__builtin_amdgcn_ballot_w64(old == nonce) != 0) ++nonce;
        if (lane == 0) __hip_atomic_store(sy + blockIdx.x * 128 + 64, nonce, RLX_AGENT);
      }
      if (isx) wait_bar<7>(); else wait_bar<8>();
      wait_bar<0>();
      BAR(); issue(0, ldsb + LDS_R0);
      BAR(); issue(1, ldsb + LDS_R1);
      if (isx) wait_bar<6>(); else wait_bar<8>();
      u32x4 junk[3] = {};
      unsigned junkt = 0;
      {
        const char* pa = ws + WS_O + ((((size_t)br * NB + b) * 4 + (lane & 3)) * 256 + oct * 64 + hpl * 16) * 1024;
        if (lane == 4) pa = ws + WS_P + (((size_t)br * NB + b) * 4 + hpl) * 262144 + ((((oct + 1) & 3) * 4 + oct) * 4) * 4096;
        if (lane == 5) pa = (const char*)(out + (size_t)b * SQ * 1024 + br * 512 + hpl * 128 + oct * 32);
        asm volatile("global_load_dword %0, %1, off" : "+v"(junkt) : "v"(pa) : "memory");
      }
#pragma nounroll
      for (unsigned j = 2; j < 8; ++j) {
        BAR(); issue(j, ldsb + LDS_R0 + (j & 1) * PBUF);
        {
          const unsigned s0 = (128 * b + (w - 10) * 64 + (j < 7 ? (j - 2) * 12 : 52)) * 1024;
          asm volatile("buffer_load_dwordx4 %0, %3, %4, %5 offen\n\tbuffer_load_dwordx4 %1, %3, %4, %6 offen\n\tbuffer_load_dwordx4 %2, %3, %4, %7 offen"
                       : "+v"(junk[0]), "+v"(junk[1]), "+v"(junk[2]) : "v"(vO), "s"(rO), "s"(s0), "s"(s0 + 4096), "s"(s0 + 8192) : "memory");
        }
        if (isx) wait_bar<6 + 3 * WARMO>(); else wait_bar<8 + 3 * WARMO>();
      }
      BAR(); wait_bar<0>();
      asm volatile("" :: "v"(junk[0]), "v"(junk[1]), "v"(junk[2]));
      asm volatile("" :: "v"(junkt));
    }
    auto issueO = [&](unsigned e) {
      const unsigned t = e >> 1, hl = 2 * ((hpl + t) & 3) + ((t >> 2) ^ par);
      const unsigned s0 = (hl * 128 + (e & 1) * 64 + (w - 10) * 32) * 1024, m0 = ldsb + wo_slot(e) + (w - 10) * 8192;
      dma4(rO, vO, s0, s0 + 3072, s0 + 6144, s0 + 9216, m0);
      dma4(rO, vO, s0 + 16384, s0 + 16384 + 3072, s0 + 16384 + 6144, s0 + 16384 + 9216, m0 + 4096);
    };
    const unsigned mbR = xcd + 8 * (4 * b + (lane & 3)), mbC = 4 * br + (lane & 3) + 8 * idx;
    unsigned echo = 0;
    auto refresh = [&](unsigned stage, bool wait) {
      const unsigned me = stage < 2 ? hpl : oct, mb = stage < 2 ? mbR : mbC;
#pragma nounroll
      for (unsigned spin = 0; spin < (1u << 16); ++spin) {
        const unsigned ack = __hip_atomic_load(sy + blockIdx.x * 128 + 4 * stage + (lane & 3), RLX_AGENT);
        const unsigned e2 = __hip_atomic_load(sy + mb * 128 + 64, RLX_AGENT);
        if (lane < 4 && lane != me && e2 != echo) __hip_atomic_store(sy + mb * 128 + 4 * stage + me, e2, RLX_AGENT);
        echo = e2;
        if (!wait || __builtin_amdgcn_ballot_w64(ack != nonce && (lane & 3) != me) == 0) break;
        __builtin_amdgcn_s_sleep(1);
      }
      __builtin_amdgcn_fence(__ATOMIC_ACQUIRE, "wavefront");
    };
    unsigned echoC = 0, ack0 = 0;
    BAR(); issueO(0); issueO(1); issueO(2); issueO(3);
    BAR();
    if (isx) { echo = __hip_atomic_load(sy + mbR * 128 + 64, RLX_AGENT); echoC = __hip_atomic_load(sy + mbC * 128 + 64, RLX_AGENT); }
    BAR();
    wait_bar<16>(); BAR();
    BAR();
    BAR(); issueO(4); issueO(5);
    BAR(); BAR(); BAR();
    BAR();
    if (isx) { if (lane < 4 && lane != hpl) __hip_atomic_store(sy + mbR * 128 + hpl, echo, RLX_AGENT); }
    BAR(); BAR();
    BAR(); issueO(8);
    BAR(); issueO(9);
    if (isx) ack0 = __hip_atomic_load(sy + blockIdx.x * 128 + (lane & 3), RLX_AGENT);
    BAR();
    BAR();
    if (isx) { if (__builtin_amdgcn_ballot_w64(ack0 != nonce && (lane & 3) != hpl) != 0) refresh(0, true); }
    issueO(6); issueO(7);
    wait_bar<16>(); issueO(10);
    if (isx) refresh(0, false);
    wait_bar<8>();
    if (isx) { if (lane < 4 && lane != hpl) __hip_atomic_store(sy + mbR * 128 + 4 + hpl, echo, RLX_AGENT); }
    issueO(13); issueO(11); issueO(14); issueO(12); issueO(15);
    if (isx) refresh(1, true);
    wait_bar<0>();
    if (!isx) return;
    BAR();
    BAR();
    echo = echoC;
    if (lane < 4 && lane != oct) __hip_atomic_store(sy + mbC * 128 + 8 + oct, echo, RLX_AGENT);
    refresh(2, true);
    BAR();
    refresh(2, false);
    return;
  }
  if (w >= 8) {
    const unsigned kbase = l4 * 256 + 16 * (l15 ^ l4);
    const unsigned vV = l4 * 256 + 16 * (l15 ^ (l4 << 2));
    const size_t kvoff = ((size_t)b * NH + 2 * hp) * SK * DH;
    const rsrc_t rK = mkrsrc((br ? kB : kA) + kvoff, 2 * SK * DH * 2);
    const rsrc_t rV = mkrsrc((br ? vB : vA) + kvoff, 2 * SK * DH * 2);
#define KVMASK 7
    const unsigned rotk = ROTK ? b + hp : 0, half = (w - 8) * 8192;
    unsigned slot = ldsb + half;
    auto issue = [&](unsigned n) {
      const unsigned so = ((n >> 4) ^ par) * 8 * SLOT + (((n >> 1) + rotk) & KVMASK) * SLOT + half;
      if (n & 1) { dma4_kv(rV, vV, vV, vV, vV, so, slot); dma4_kv(rV, vV, vV, vV, vV, so + 4096, slot + 4096); }
      else { dma4_kv(rK, kbase, kbase ^ 64, kbase ^ 128, kbase ^ 192, so, slot); dma4_kv(rK, kbase, kbase ^ 64, kbase ^ 128, kbase ^ 192, so + 4096, slot + 4096); }
      slot += SLOT; if (slot >= ldsb + 6 * SLOT) slot = ldsb + half;
    };
    u32x4 junk = {};
    {
      const char* pc = (const char*)(pc0 & ~(unsigned long long)255) + (w - 8) * 1024;
#pragma nounroll
      for (unsigned k = 0; k < CODE_KB / 2; ++k) asm volatile("global_load_dwordx4 %0, %1, %2" : "+v"(junk) : "v"(lane * 16 + k * 2048), "s"(pc) : "memory");
    }
    if (KV_A < -1) { issue(0); issue(1); }
    BAR();
#pragma nounroll
    for (int i = 0; i < 8; ++i) {
      BAR();
      if (i == KV_A) { issue(0); issue(1); }
      if (i == 5) { if (KVL > 2) issue(2); }
    }
    static_assert(KVL == 2 || KVL == 3, "ring depth");
    wait_bar<8 * (KVL - 1)>(); issue(KVL);
#pragma nounroll
    for (unsigned n = 1; n < 29; n += 2) { BAR(); issue(n + KVL); wait_bar<8 * (KVL - 1)>(); issue(n + 1 + KVL); }
    BAR(); if (KVL == 2) issue(31);
    wait_bar<8>(); BAR();
    wait_bar<0>();
    asm volatile("" :: "v"(junk));
    return;
  }

  const unsigned r32 = lane & 31, hi = lane >> 5, g = w >> 2, wq = w & 3;
  STAMP_DECL; STAMPRT(62); STAMP(0);
  const unsigned tq = l15 >> 2, tp = lane & 3, blk = l4 & 1;
  const unsigned tr0 = 256 * tq + 64 * tq + 32 * blk + 16 * (tp & 1) + 8 * (tp >> 1);
  const unsigned xrow = (32 * wq + r32 < SQ) ? 32 * wq + r32 : 32 * wq + r32 - 16;
  const unsigned xa0 = 16384 + 128 * xrow + 16 * (hi ^ ((xrow >> 1) & 7));
  const unsigned ka0 = 256 * r32 + 16 * (hi ^ (r32 & 15));

  f32x16 acc[4];
#pragma unroll
  for (int c = 0; c < 4; ++c) acc[c] = f32x16{};
  const rsrc_t rW = mkrsrc((br ? wqB : wqA) + (size_t)hp * 256, (DM * HD - hp * 256) * 2);
  auto issueW = [&](unsigned gg, unsigned u, unsigned buf) {
    const unsigned s0 = (gg ^ par) * 256 + (64 * ((u + rotq) & 7) + 16 * wq) * 8192;
    dma4(rW, l4 * 8192 + 16 * (l15 ^ (l4 << 2)), s0, s0 + 31744, s0 + 2 * 31744, s0 + 3 * 31744, buf + wq * 4096);
  };
#define QFIRST(wb, xb) do { xf[0] = ldsfrag(xb); _Pragma("unroll") for (int i = 0; i < 4; ++i) { lo[i] = trread((wb) ^ (64 * i)); hi_[i] = trread(((wb) ^ (64 * i)) + 1024); } } while (0)
#define QGAP(i) do { acc[(i) & 3] = MFMA(cat(lo[i], hi_[i]), xf[(i) >> 2], acc[(i) & 3]); \
    if ((i) + 4 < 16) { if (((i) & 3) == 0) xf[((i) >> 2) + 1] = ldsfrag(xb ^ (32 * (((i) >> 2) + 1))); \
      lo[(i) + 4] = trread((wb ^ (64 * ((i) & 3))) + 4096 * (((i) + 4) >> 2)); hi_[(i) + 4] = trread((wb ^ (64 * ((i) & 3))) + 4096 * (((i) + 4) >> 2) + 1024); } } while (0)
  if (g) {
    issueW(0, 0, ldsb + 2 * SLOT); issueW(0, 1, ldsb + 4 * SLOT);
#pragma nounroll
    for (unsigned i = 2; i < 8; ++i) { wait_bar<4>(); issueW(0, i, ldsb + (2 + 2 * (i & 3)) * SLOT); }
    wait_bar<4>(); wait_bar<0>();
    BAR(); issueW(1, 0, ldsb + LDS_R0);
    BAR(); issueW(1, 1, ldsb + LDS_R1);
#pragma nounroll
    for (unsigned u = 0; u < 8; ++u) {
      const unsigned buf = ldsb + LDS_R0 + (u & 1) * PBUF, wb = buf + hi * 2048 + tr0, xb = buf + xa0;
      bf16x8 xf[4]; s16x4 lo[16], hi_[16];
      if (u < 7) wait_bar<4>(); else wait_bar<0>();
      STAMP(1 + u);
#pragma unroll
      for (int i = 0; i < 16; ++i) {
        if ((i & 3) == 0) xf[i >> 2] = ldsfrag(xb ^ (32 * (i >> 2)));
        lo[i] = trread((wb ^ (64 * (i & 3))) + 4096 * (i >> 2)); hi_[i] = trread((wb ^ (64 * (i & 3))) + 4096 * (i >> 2) + 1024);
      }
      SBAR();
#pragma unroll
      for (int i = 0; i < 8; ++i) { acc[i & 3] = MFMA(cat(lo[i], hi_[i]), xf[i >> 2], acc[i & 3]); SBAR(); }
      if (u < 7) LBAR();
      if (u < 6) issueW(1, u + 2, buf);
      SBAR();
#pragma unroll
      for (int i = 8; i < 16; ++i) { acc[i & 3] = MFMA(cat(lo[i], hi_[i]), xf[i >> 2], acc[i & 3]); SBAR(); }
    }
    BAR();
  } else {
    bf16x8 xf[4]; s16x4 lo[16], hi_[16];
    unsigned wb = ldsb + 2 * SLOT + hi * 2048 + tr0, xb = ldsb + 2 * SLOT + xa0;
    BAR();
    QFIRST(wb, xb);
#pragma nounroll
    for (unsigned u = 0; u < 8; ++u) {
      STAMP(1 + u);
      const unsigned d = (u & 3) == 3 ? -3 * PBUF : PBUF, wn = wb + d, xn = xb + d;
      SBAR();
#pragma unroll
      for (int i = 0; i < 16; ++i) {
        QGAP(i);
        if (i >= 12) { if (i == 12) xf[0] = ldsfrag(xn); lo[i - 12] = trread(wn ^ (64 * (i & 3))); hi_[i - 12] = trread((wn ^ (64 * (i & 3))) + 1024); }
        SBAR();
        if (i == 11) { BAR(); SBAR(); }
      }
      wb = wn; xb = xn;
    }
  }
  bf16x8 qf[8];
#pragma unroll
  for (int c = 0; c < 4; ++c) { acc[c] *= SM_C; qf[2 * c] = pack8(acc[c], 0); qf[2 * c + 1] = pack8(acc[c], 8); }
  if (32 * wq + r32 >= SQ) {
#pragma unroll
    for (int c = 0; c < 8; ++c) qf[c] = bf16x8{};
  }

#pragma unroll
  for (int c = 0; c < 4; ++c) acc[c] = f32x16{};
  float m = 0.f, l = 0.f, lo_thr = 0.f;
  unsigned ksl = ldsb + (g ? 4 * SLOT : 0), vsl = ksl;
  STAMP(9);
  float l2 = 0.f;
  f32x16 s0, s1; unsigned pA[8], pB[8];
#define PFR(P, k) __builtin_bit_cast(bf16x8, u32x4{P[4 * (k)], P[4 * (k) + 1], P[4 * (k) + 2], P[4 * (k) + 3]})
  s16x4 vlo[8], vhi[8]; bf16x8 kf[8];
#define NEXTSLOT(x) ((x) + SLOT == ldsb + 6 * SLOT ? ldsb : (x) + SLOT)
#define VRD(i, vb, vo) do { vlo[i] = trread(((vb) ^ (64 * ((i) & 3))) + (vo) + 4096 * ((i) >> 2)); vhi[i] = trread(((vb) ^ (64 * ((i) & 3))) + (vo) + 4096 * ((i) >> 2) + 2048); } while (0)
#define ELO(i) ((i) < 4 ? 0 : (16 * ((i) - 4)) / 12)
#define HSTEP(DOPV, DOQK, SC, SN, PI, PO, vb, vo, kb, ko, vn, vno) do { \
    float mx0, mx1, alpha = 1.f, pe[16]; bool pend = false; \
    SBAR(); \
    _Pragma("unroll") for (int i = 0; i < 16; ++i) { \
      if (i < 8) { if (DOPV) acc[i & 3] = MFMA(cat(vlo[i], vhi[i]), PFR(PI, i >> 2), acc[i & 3]); } \
      else if (DOQK) { if (i == 8) SN = MFMA(kf[0], qf[0], f32x16{}); else SN = MFMA(kf[i - 8], qf[i - 8], SN); } \
      if (i & 1) { _Pragma("unroll") for (int f = i + 4; f >= i + 3; --f) {     \
          if (f < 8) { if (DOPV) VRD(f, vb, vo); } else if (f < 16) { if (DOQK) kf[f - 8] = ldsfrag(((kb) ^ (32 * (f - 8))) + (ko)); } else VRD(f - 16, vn, vno); } } \
      if (i == 1) { mx0 = max3f(SC[0], SC[1], SC[2]); mx1 = max3f(SC[8], SC[9], SC[10]); mx0 = max3f(mx0, SC[3], SC[4]); mx1 = max3f(mx1, SC[11], SC[12]); PIN(mx0); PIN(mx1); } \
      if (i == 2) { mx0 = max3f(SC[5], SC[6], mx0); mx1 = max3f(SC[13], SC[14], mx1); PIN(mx0); PIN(mx1); mx0 = max3f(SC[7], SC[15], mx0); PIN(mx0); } \
      if (i == 3) {     \
        if (__builtin_expect(__builtin_amdgcn_ballot_w64(!(max3v(mx0, mx1, lo_thr) < SM_HI)) != 0, 0)) { \
          lo_thr = SM_HI; mx0 = __builtin_fmaxf(mx0, mx1); \
          if (__builtin_amdgcn_ballot_w64(!(mx0 - m <= SM_THR)) != 0) {     \
            auto sw = __builtin_amdgcn_permlane32_swap(__float_as_uint(mx0), __float_as_uint(mx0), false, false); \
            const float mn = max3f(m, __uint_as_float(sw[0]), __uint_as_float(sw[1])); alpha = __builtin_amdgcn_exp2f(m - mn); m = mn; l *= alpha; l2 *= alpha; pend = true; } \
          _Pragma("unroll") for (int e = 0; e < 16; ++e) SC[e] -= m; } } \
      if (i >= 5) { _Pragma("unroll") for (int e = ELO(i - 1); e < ELO(i); ++e) { if (e & 1) { l2 += pe[e]; PO[e >> 1] = cvtpk(pe[e - 1], pe[e]); PIN(PO[e >> 1]); } else l += pe[e]; } PIN(l); PIN(l2); } \
      if (i >= 4) { _Pragma("unroll") for (int e = ELO(i); e < ELO(i + 1); ++e) { pe[e] = __builtin_amdgcn_exp2f(SC[e]); PIN(pe[e]); } } \
      SBAR(); \
      if (i == 7) { if (__builtin_expect(pend, 0)) { _Pragma("unroll") for (int c = 0; c < 4; ++c) acc[c] *= alpha; } SBAR(); } \
      if (i == 12) { BAR(); SBAR(); } \
    } \
    _Pragma("unroll") for (int e = ELO(15); e < 16; ++e) { if (e & 1) { l2 += pe[e]; PO[e >> 1] = cvtpk(pe[e - 1], pe[e]); } else l += pe[e]; } \
    PIN(PO[7]); PIN(l); PIN(l2); \
  } while (0)
  if (!g) LBAR();
  {
    const unsigned kb = ksl + ka0;
#pragma unroll
    for (int i = 0; i < 8; ++i) kf[i] = ldsfrag(kb ^ (32 * i));
    s0 = MFMA(kf[0], qf[0], f32x16{});
#pragma unroll
    for (int i = 1; i < 8; ++i) s0 = MFMA(kf[i], qf[i], s0);
    {
      float mx = max3f(s0[0], s0[1], s0[2]);
#pragma unroll
      for (int e = 3; e < 15; e += 2) mx = max3f(mx, s0[e], s0[e + 1]);
      mx = __builtin_fmaxf(mx, s0[15]);
      auto sw = __builtin_amdgcn_permlane32_swap(__float_as_uint(mx), __float_as_uint(mx), false, false);
      mx = __builtin_fmaxf(__uint_as_float(sw[0]), __uint_as_float(sw[1]));
      if (__builtin_amdgcn_ballot_w64(!(mx >= -SM_LO)) != 0) { lo_thr = SM_HI; m = mx; }
    }
    SBAR();
    BAR();
  }
  {
    STAMP(10);
    vsl = NEXTSLOT(ksl);
    const unsigned kb0 = ksl + ka0, vb1 = vsl + hi * 1024 + tr0;
    HSTEP(false, true, s0, s1, pB, pA, vb1, 0, kb0, 8192, vb1, 0);
    ksl = NEXTSLOT(vsl);
  }
#pragma nounroll
  for (unsigned j = 0; j < 7; ++j) {
    const unsigned vnext = NEXTSLOT(ksl);
    const unsigned vb1 = vsl + hi * 1024 + tr0, kb1 = ksl + ka0, vb2 = vnext + hi * 1024 + tr0;
    HSTEP(true, true, s1, s0, pA, pB, vb1, 0, kb1, 0, vb1, 8192);
    STAMP(14 + 4 * j);
    unsigned kb2 = kb1, vb1b = vb1; PIN(kb2); if (!KEEPV) PIN(vb1b);
    HSTEP(true, true, s0, s1, pB, pA, vb1b, 8192, kb2, 8192, vb2, 0);
    vsl = vnext; ksl = NEXTSLOT(vnext);
  }
  {
    const unsigned vb1 = vsl + hi * 1024 + tr0;
    HSTEP(true, false, s1, s0, pA, pB, vb1, 0, vb1, 0, vb1, 8192);
  }
  {
    const unsigned vb = vsl + hi * 1024 + tr0;
    SBAR();
#pragma unroll
    for (int i = 0; i < 8; ++i) {
      acc[i & 3] = MFMA(cat(vlo[i], vhi[i]), PFR(pB, i >> 2), acc[i & 3]);
      if (i < 4) VRD(i + 4, vb, 8192);
      SBAR();
    }
  }
  STAMP(46);
  unsigned laneT; asm volatile("v_mbcnt_lo_u32_b32 %0, -1, 0\n\tv_mbcnt_hi_u32_b32 %0, -1, %0" : "=v"(laneT));
  const unsigned r32T = laneT & 31, hiT = laneT >> 5, tqT = (laneT & 15) >> 2, tpT = laneT & 3, blkT = (laneT >> 4) & 1;
  const unsigned vo16 = (32 * wq + r32T < SQ) ? laneT * 16 : 0x80000000u;
  const rsrc_t rOf = mkrsrc(ws + WS_O + ((((size_t)br * NB + b) * 4 + wq) * 256 + oct * 64) * 1024, 65536);
  bf16x8 of[8];
  {
    const float inv = 1.f / swap32_add(l + l2);
    __builtin_amdgcn_s_setprio(3);
#pragma unroll
    for (int c = 0; c < 4; ++c) {
      acc[c] *= inv; of[2 * c] = pack8(acc[c], 0); of[2 * c + 1] = pack8(acc[c], 8);
      __builtin_amdgcn_raw_buffer_store_b128(__builtin_bit_cast(u32x4, of[2 * c]), rOf, vo16, (2 * hpl + (g ^ par)) * 8192 + 2 * c * 1024, OPOL);
      __builtin_amdgcn_raw_buffer_store_b128(__builtin_bit_cast(u32x4, of[2 * c + 1]), rOf, vo16, (2 * hpl + (g ^ par)) * 8192 + (2 * c + 1) * 1024, OPOL);
      if (c == 1 && !g) BAR();
    }
    __builtin_amdgcn_s_setprio(0);
  }
  STAMP(47);
#pragma unroll
  for (int c = 0; c < 4; ++c) acc[c] = f32x16{};
  const unsigned tro = ldsb + hiT * 2048 + 256 * tqT + 64 * tqT + 32 * (tpT & 1) + 16 * blkT + 8 * (tpT >> 1);
#define NBAR(n) do { _Pragma("nounroll") for (unsigned i_ = (n); i_ > 0; --i_) LBAR(); } while (0)
#define OITEM(F, fo, e) do { const unsigned wb = wo_slot(e) + tro; s16x4 lo[16], hi_[16]; SBAR(); \
    _Pragma("unroll") for (int i = 0; i < 4; ++i) { lo[i] = trread(wb ^ (64 * i)); hi_[i] = trread((wb ^ (64 * i)) + 1024); } \
    SBAR(); \
    _Pragma("unroll") for (int i = 0; i < 16; ++i) {     \
      acc[i & 3] = MFMA(cat(lo[i], hi_[i]), F[(fo) + (i >> 2)], acc[i & 3]); \
      if (i + 4 < 16) { lo[i + 4] = trread((wb ^ (64 * (i & 3))) + 4096 * ((i + 4) >> 2)); hi_[i + 4] = trread((wb ^ (64 * (i & 3))) + 4096 * ((i + 4) >> 2) + 1024); } \
      SBAR(); } } while (0)
  NBAR(g ? 0 : 1); OITEM(of, 0, 8 * g);
  NBAR(g ? 0 : 2); OITEM(of, 4, 8 * g + 1);
  STAMP(49);
  NBAR(g ? 0 : 4);
  wait_bar<0>();
#pragma nounroll
  for (unsigned r = g ? 2 : 0; r < 3; ++r) {
    NBAR(r == 0 ? 7 : r == 1 ? 0 : 1);
    const unsigned e0 = r == 2 ? 10 + 3 * g : 2 + 3 * r;
    bf16x8 pf[12];
#pragma unroll
    for (int k = 0; k < 3; ++k) {
      const unsigned e = e0 + k, t = e >> 1, so = (2 * ((hpl + t) & 3) + ((t >> 2) ^ par)) * 8192 + (e & 1) * 4096;
#pragma unroll
      for (int s = 0; s < 4; ++s) pf[4 * k + s] = __builtin_bit_cast(bf16x8, __builtin_amdgcn_raw_buffer_load_b128(rOf, vo16, so + s * 1024, 16));
    }
    STAMP(50 + 2 * r);
    NBAR(r == 1);
    OITEM(pf, 0, e0); OITEM(pf, 4, e0 + 1); OITEM(pf, 8, e0 + 2);
    STAMP(51 + 2 * r);
  }
  {
    LAS f32x4* xw = (LAS f32x4*)(size_t)(ldsb + 4 * SLOT + w * 8192 + laneT * 16);
    const LAS f32x4* xr = (const LAS f32x4*)(size_t)(ldsb + 4 * SLOT + (w ^ 4) * 8192 + laneT * 16);
#define XSEND(A, c) _Pragma("unroll") for (int q4 = 0; q4 < 4; ++q4) xw[((c) * 4 + q4) * 64] = f32x4{A[4 * q4], A[4 * q4 + 1], A[4 * q4 + 2], A[4 * q4 + 3]}
    if (g) { XSEND(acc[0], 0); XSEND(acc[1], 1); acc[0] = acc[2]; acc[1] = acc[3]; } else { XSEND(acc[2], 0); XSEND(acc[3], 1); }
    LBAR();
#pragma unroll
    for (int c = 0; c < 2; ++c)
#pragma unroll
      for (int q4 = 0; q4 < 4; ++q4) { const f32x4 o = xr[(c * 4 + q4) * 64];
#pragma unroll
        for (int k = 0; k < 4; ++k) acc[c][4 * q4 + k] += o[k]; }
  }
  STAMP(57);
  const rsrc_t rP = mkrsrc(ws + WS_P + (((size_t)br * NB + b) * 4 + hpl) * 262144, 262144);
#pragma unroll
  for (int c = 0; c < 2; ++c) {
    if (2 * g + c != oct) {
      __builtin_amdgcn_raw_buffer_store_b128(__builtin_bit_cast(u32x4, pack8(acc[c], 0)), rP, vo16, (((2 * g + c) * 4 + oct) * 4 + wq) * 4096, 16);
      __builtin_amdgcn_raw_buffer_store_b128(__builtin_bit_cast(u32x4, pack8(acc[c], 8)), rP, vo16, (((2 * g + c) * 4 + oct) * 4 + wq) * 4096 + 1024, 16);
    }
  }
  if (oct & 1) acc[0] = acc[1];
  VM0();
  STAMP(58);
  BAR();
  if (g != (oct >> 1)) return;
  BAR();
  STAMP(59);
  if (g == (oct >> 1)) {
    u32x4 pv[6];
#pragma unroll
    for (int k = 0; k < 3; ++k)
#pragma unroll
      for (int h2 = 0; h2 < 2; ++h2)
        pv[2 * k + h2] = __builtin_amdgcn_raw_buffer_load_b128(rP, vo16, ((oct * 4 + ((oct + 1 + k) & 3)) * 4 + wq) * 4096 + h2 * 1024, 16);
#pragma unroll
    for (int k = 0; k < 3; ++k)
#pragma unroll
      for (int r = 0; r < 16; ++r) { const unsigned u = pv[2 * k + (r >> 3)][(r >> 1) & 3]; acc[0][r] += __uint_as_float((r & 1) ? (u & 0xffff0000u) : (u << 16)); }
    const unsigned q = 32 * wq + r32T;
    if (q < SQ) {
      u16* o = out + ((size_t)b * SQ + q) * 1024 + br * 512 + hpl * 128 + oct * 32 + hiT * 16;
      *(bf16x8*)o = pack8(acc[0], 0);
      *(bf16x8*)(o + 8) = pack8(acc[0], 8);
    }
  }
  STAMP(48); STAMPRT(63); STAMP_OUT(0);
}

extern "C" void kernel_launch(void* const* d_in, const int* in_sizes, int n_in, void* d_out, int out_size, void* d_ws, size_t ws_size, hipStream_t stream) {
  const u16* xA = (const u16*)d_in[0]; const u16* kA = (const u16*)d_in[1]; const u16* vA = (const u16*)d_in[2]; const u16* wqA = (const u16*)d_in[3]; const u16* woA = (const u16*)d_in[4];
  const u16* xB = (const u16*)d_in[5]; const u16* kB = (const u16*)d_in[6]; const u16* vB = (const u16*)d_in[7]; const u16* wqB = (const u16*)d_in[8]; const u16* woB = (const u16*)d_in[9];
  (void)hipFuncSetAttribute((const void*)fused_kernel, hipFuncAttributeMaxDynamicSharedMemorySize, LDS_BYTES);
  fused_kernel<<<256, NTHREADS, LDS_BYTES, stream>>>(xA, kA, vA, wqA, woA, xB, kB, vB, wqB, woB, (char*)d_ws, (u16*)d_out);
}
```

```cpp
#include <hip/hip_runtime.h>
#include <hip/hip_bf16.h>

typedef __bf16 bf16x8 __attribute__((ext_vector_type(8)));
typedef __bf16 bf16x2 __attribute__((ext_vector_type(2)));
typedef short s16x4 __attribute__((ext_vector_type(4)));
typedef short s16x8 __attribute__((ext_vector_type(8)));
typedef float f32x16 __attribute__((ext_vector_type(16)));
typedef float f32x4 __attribute__((ext_vector_type(4)));
typedef float f32x2 __attribute__((ext_vector_type(2)));
typedef unsigned u32x4 __attribute__((ext_vector_type(4)));
typedef unsigned short u16;
#define LAS __attribute__((address_space(3)))
#define DI __device__ __forceinline__
#define MFMA(a, b, c) __builtin_amdgcn_mfma_f32_32x32x16_bf16((a), (b), (c), 0, 0, 0)

constexpr int NB = 8, SQ = 112, DM = 512, NH = 32, SK = 512, DH = 128, HD = NH * DH;
constexpr unsigned SLOT = 16384;
constexpr int LDS_BYTES = 163840;
constexpr float SM_C = 0.08838834764831845f * 1.4426950408889634f;
constexpr float SM_HI = 30.f, SM_LO = 30.f;
constexpr float SM_THR = 8.f;

constexpr size_t WS_SY = 0;
constexpr size_t WS_O = 262144;
constexpr size_t WS_O_BYTES = (size_t)2 * NB * 4 * 256 * 1024;
constexpr size_t WS_P = WS_O + WS_O_BYTES;

DI unsigned rfl(unsigned x) { return __builtin_amdgcn_readfirstlane(x); }
DI unsigned cvtpk(float lo, float hi) { f32x2 v = {lo, hi}; bf16x2 b = __builtin_convertvector(v, bf16x2); return __builtin_bit_cast(unsigned, b); }
DI bf16x8 pack8(const f32x16& x, int base) {
  u32x4 w = {cvtpk(x[base + 0], x[base + 1]), cvtpk(x[base + 2], x[base + 3]), cvtpk(x[base + 4], x[base + 5]), cvtpk(x[base + 6], x[base + 7])};
  return __builtin_bit_cast(bf16x8, w);
}
DI s16x4 trread(unsigned a) { return __builtin_amdgcn_ds_read_tr16_b64_v4i16((LAS s16x4*)(size_t)a); }
DI bf16x8 cat(s16x4 lo, s16x4 hi) { s16x8 r = __builtin_shufflevector(lo, hi, 0, 1, 2, 3, 4, 5, 6, 7); return __builtin_bit_cast(bf16x8, r); }
DI bf16x8 ldsfrag(unsigned a) { return *(const LAS bf16x8*)(size_t)a; }
DI float swap32_add(float x) { auto r = __builtin_amdgcn_permlane32_swap(__float_as_uint(x), __float_as_uint(x), false, false); return __uint_as_float(r[0]) + __uint_as_float(r[1]); }
DI float max3f(float a, float b, float c) { return __builtin_fmaxf(__builtin_fmaxf(a, b), c); }
DI float max3v(float a, float b, float c) { float r; asm("v_max3_f32 %0, %1, %2, %3" : "=v"(r) : "v"(a), "v"(b), "v"(c)); return r; }

typedef __amdgpu_buffer_rsrc_t rsrc_t;
DI rsrc_t mkrsrc(const void* p, unsigned bytes) { return __builtin_amdgcn_make_buffer_rsrc((void*)p, (short)0, (int)bytes, 0x00020000); }

DI void dma4(rsrc_t r, unsigned voff, unsigned s0, unsigned s1, unsigned s2, unsigned s3, unsigned m0v) {
  unsigned keep;
  asm volatile("s_mov_b32 %0, m0\n\ts_mov_b32 m0, %7\n\ts_nop 0\n\t"
               "buffer_load_dwordx4 %1, %2, %3 offen lds\n\t"
               "buffer_load_dwordx4 %1, %2, %4 offen offset:1024 lds\n\t"
               "buffer_load_dwordx4 %1, %2, %5 offen offset:2048 lds\n\t"
               "buffer_load_dwordx4 %1, %2, %6 offen offset:3072 lds\n\t"
               "s_mov_b32 m0, %0"
               : "=&s"(keep) : "v"(voff), "s"(r), "s"(s0), "s"(s1), "s"(s2), "s"(s3), "s"(m0v) : "memory");
}
DI void dma4_kv(rsrc_t r, unsigned v0, unsigned v1, unsigned v2, unsigned v3, unsigned soff, unsigned m0v) {
  unsigned keep;
  asm volatile("s_mov_b32 %0, m0\n\ts_mov_b32 m0, %7\n\ts_nop 0\n\t"
               "buffer_load_dwordx4 %1, %5, %6 offen nt lds\n\t"
               "buffer_load_dwordx4 %2, %5, %6 offen offset:1024 nt lds\n\t"
               "buffer_load_dwordx4 %3, %5, %6 offen offset:2048 nt lds\n\t"
               "buffer_load_dwordx4 %4, %5, %6 offen offset:3072 nt lds\n\t"
               "s_mov_b32 m0, %0"
               : "=&s"(keep) : "v"(v0), "v"(v1), "v"(v2), "v"(v3), "s"(r), "s"(soff), "s"(m0v) : "memory");
}
template <int N> DI void wait_bar() { asm volatile("s_waitcnt vmcnt(%0) lgkmcnt(0)\n\ts_barrier" :: "i"(N) : "memory"); }
#define SBAR() __builtin_amdgcn_sched_barrier(0)
#define BAR() asm volatile("s_barrier" ::: "memory")
#define LBAR() asm volatile("s_waitcnt lgkmcnt(0)\n\ts_barrier" ::: "memory")
#define PIN(x) asm volatile("" : "+v"(x))


constexpr unsigned CODE_KB = 20;
constexpr int NTHREADS = 768;
constexpr unsigned PBUF = 32768;
constexpr unsigned LDS_R0 = 6 * SLOT, LDS_R1 = 8 * SLOT;

DI void dma2(rsrc_t r, unsigned v0, unsigned v1, unsigned s0, unsigned s1, unsigned m0v) {
  unsigned keep;
  asm volatile("s_mov_b32 %0, m0\n\ts_mov_b32 m0, %6\n\ts_nop 0\n\t"
               "buffer_load_dwordx4 %1, %3, %4 offen lds\n\t"
               "buffer_load_dwordx4 %2, %3, %5 offen offset:1024 lds\n\t"
               "s_mov_b32 m0, %0"
               : "=&s"(keep) : "v"(v0), "v"(v1), "s"(r), "s"(s0), "s"(s1), "s"(m0v) : "memory");
}
typedef __attribute__((address_space(1))) unsigned gu32;
#define RLX_AGENT __ATOMIC_RELAXED, __HIP_MEMORY_SCOPE_AGENT
#define VM0() asm volatile("s_waitcnt vmcnt(0)" ::: "memory")
DI unsigned wo_slot(unsigned e) { return (unsigned)((0x3982103254769876ull >> (4 * e)) & 15) * SLOT; }

__global__ __launch_bounds__(NTHREADS, 3) void fused_kernel(const u16* __restrict__ xA, const u16* __restrict__ kA, const u16* __restrict__ vA, const u16* __restrict__ wqA, const u16* __restrict__ woA,
                                                            const u16* __restrict__ xB, const u16* __restrict__ kB, const u16* __restrict__ vB, const u16* __restrict__ wqB, const u16* __restrict__ woB,
                                                            char* __restrict__ ws, u16* __restrict__ out) {
  extern __shared__ __attribute__((aligned(16))) char lds[];
  unsigned long long pc0; asm volatile("s_getpc_b64 %0" : "=s"(pc0));
  const unsigned tid = threadIdx.x, lane = tid & 63;
  const unsigned w = rfl(tid >> 6);
  const unsigned xcd = blockIdx.x & 7, idx = blockIdx.x >> 3;
  const unsigned br = xcd >> 2, oct = xcd & 3, hpl = idx & 3, hp = oct * 4 + hpl, b = idx >> 2;
  const unsigned ldsb = (unsigned)(size_t)lds;
  const unsigned l4 = lane >> 4, l15 = lane & 15;
  gu32* const sy = (gu32*)(ws + WS_SY);
  const unsigned rotq = b + 2 * hpl;

  if (__builtin_expect(w >= 10, 1)) {
    const bool isx = w == 11;
    const rsrc_t rO = mkrsrc((br ? woB : woA) + (size_t)(oct * 1024) * DM + hpl * 128, (1024 * DM - hpl * 128) * 2);
    const unsigned vO = l4 * 1024 + 16 * (l15 ^ (l4 << 2));
    unsigned nonce = 0;
    {
      const rsrc_t r = mkrsrc((br ? xB : xA) + (size_t)b * SQ * DM, SQ * DM * 2);
      const unsigned v0 = (lane >> 3) * 1024 + 16 * ((lane & 7) ^ l4), v1 = (lane >> 3) * 1024 + 16 * ((lane & 7) ^ (4 + l4));
      auto issue = [&](unsigned u, unsigned buf) {
        const unsigned s0 = ((u + rotq) & 7) * 128 + (w - 10) * 65536, m0 = buf + 16384 + (w - 10) * 8192;
        dma2(r, v0, v1, s0, s0 + 7168, m0); dma2(r, v0, v1, s0 + 16384, s0 + 16384 + 7168, m0 + 2048); dma2(r, v0, v1, s0 + 32768, s0 + 32768 + 7168, m0 + 4096);
        if (!isx) dma2(r, v0, v1, s0 + 49152, s0 + 49152 + 7168, m0 + 6144);
      };
      unsigned old;
      asm volatile("global_load_dword %0, %1, %2 sc1" : "=v"(old) : "v"((lane < 12 ? lane : 0) * 4), "s"(sy + blockIdx.x * 128) : "memory");
      issue(0, ldsb + 2 * SLOT); issue(1, ldsb + 4 * SLOT);
#pragma nounroll
      for (unsigned i = 2; i < 8; ++i) { if (isx) wait_bar<6>(); else wait_bar<8>(); issue(i, ldsb + (2 + 2 * (i & 3)) * SLOT); }
      PIN(old);
      if (isx) {
        nonce = rfl(old) + 1;
        while (__builtin_amdgcn_ballot_w64(old == nonce) != 0) ++nonce;
        if (lane == 0) __hip_atomic_store(sy + blockIdx.x * 128 + 64, nonce, RLX_AGENT);
      }
      if (isx) wait_bar<7>(); else wait_bar<8>();
      wait_bar<0>();
      BAR(); issue(0, ldsb + LDS_R0);
      BAR(); issue(1, ldsb + LDS_R1);
      if (isx) wait_bar<6>(); else wait_bar<8>();
      u32x4 junk[3] = {};
      unsigned junkt = 0;
      {
        const char* pa = ws + WS_O + ((((size_t)br * NB + b) * 4 + (lane & 3)) * 256 + oct * 64 + hpl * 16) * 1024;
        if (lane == 4) pa = ws + WS_P + (((size_t)br * NB + b) * 4 + hpl) * 262144 + ((((oct + 1) & 3) * 4 + oct) * 4) * 4096;
        if (lane == 5) pa = (const char*)(out + (size_t)b * SQ * 1024 + br * 512 + hpl * 128 + oct * 32);
        asm volatile("global_load_dword %0, %1, off" : "+v"(junkt) : "v"(pa) : "memory");
      }
#pragma nounroll
      for (unsigned j = 2; j < 8; ++j) {
        BAR(); issue(j, ldsb + LDS_R0 + (j & 1) * PBUF);
        {
          const unsigned s0 = (128 * b + (w - 10) * 64 + (j < 7 ? (j - 2) * 12 : 52)) * 1024;
          asm volatile("buffer_load_dwordx4 %0, %3, %4, %5 offen\n\tbuffer_load_dwordx4 %1, %3, %4, %6 offen\n\tbuffer_load_dwordx4 %2, %3, %4, %7 offen"
                       : "+v"(junk[0]), "+v"(junk[1]), "+v"(junk[2]) : "v"(vO), "s"(rO), "s"(s0), "s"(s0 + 4096), "s"(s0 + 8192) : "memory");
        }
        if (isx) wait_bar<6 + 3>(); else wait_bar<8 + 3>();
      }
      BAR(); wait_bar<0>();
      asm volatile("" :: "v"(junk[0]), "v"(junk[1]), "v"(junk[2]));
      asm volatile("" :: "v"(junkt));
    }
    auto issueO = [&](unsigned e) {
      const unsigned t = e >> 1, hl = 2 * ((hpl + t) & 3) + (t >> 2);
      const unsigned s0 = (hl * 128 + (e & 1) * 64 + (w - 10) * 32) * 1024, m0 = ldsb + wo_slot(e) + (w - 10) * 8192;
      dma4(rO, vO, s0, s0 + 3072, s0 + 6144, s0 + 9216, m0);
      dma4(rO, vO, s0 + 16384, s0 + 16384 + 3072, s0 + 16384 + 6144, s0 + 16384 + 9216, m0 + 4096);
    };
    const unsigned mbR = xcd + 8 * (4 * b + (lane & 3)), mbC = 4 * br + (lane & 3) + 8 * idx;
    unsigned echo = 0;
    auto refresh = [&](unsigned stage, bool wait) {
      const unsigned me = stage < 2 ? hpl : oct, mb = stage < 2 ? mbR : mbC;
#pragma nounroll
      for (unsigned spin = 0; spin < (1u << 16); ++spin) {
        const unsigned ack = __hip_atomic_load(sy + blockIdx.x * 128 + 4 * stage + (lane & 3), RLX_AGENT);
        const unsigned e2 = __hip_atomic_load(sy + mb * 128 + 64, RLX_AGENT);
        if (lane < 4 && lane != me && e2 != echo) __hip_atomic_store(sy + mb * 128 + 4 * stage + me, e2, RLX_AGENT);
        echo = e2;
        if (!wait || __builtin_amdgcn_ballot_w64(ack != nonce && (lane & 3) != me) == 0) break;
        __builtin_amdgcn_s_sleep(1);
      }
      __builtin_amdgcn_fence(__ATOMIC_ACQUIRE, "wavefront");
    };
    unsigned echoC = 0, ack0 = 0;
    BAR(); issueO(0); issueO(1); issueO(2); issueO(3);
    BAR();
    if (isx) { echo = __hip_atomic_load(sy + mbR * 128 + 64, RLX_AGENT); echoC = __hip_atomic_load(sy + mbC * 128 + 64, RLX_AGENT); }
    BAR();
    wait_bar<16>(); BAR();
    BAR();
    BAR(); issueO(4); issueO(5);
    BAR(); BAR(); BAR();
    BAR();
    if (isx) { if (lane < 4 && lane != hpl) __hip_atomic_store(sy + mbR * 128 + hpl, echo, RLX_AGENT); }
    BAR(); BAR();
    BAR(); issueO(8);
    BAR(); issueO(9);
    if (isx) ack0 = __hip_atomic_load(sy + blockIdx.x * 128 + (lane & 3), RLX_AGENT);
    BAR();
    BAR();
    if (isx) { if (__builtin_amdgcn_ballot_w64(ack0 != nonce && (lane & 3) != hpl) != 0) refresh(0, true); }
    issueO(6); issueO(7);
    wait_bar<16>(); issueO(10);
    if (isx) refresh(0, false);
    wait_bar<8>();
    if (isx) { if (lane < 4 && lane != hpl) __hip_atomic_store(sy + mbR * 128 + 4 + hpl, echo, RLX_AGENT); }
    issueO(13); issueO(11); issueO(14); issueO(12); issueO(15);
    if (isx) refresh(1, true);
    wait_bar<0>();
    if (!isx) return;
    BAR();
    BAR();
    echo = echoC;
    if (lane < 4 && lane != oct) __hip_atomic_store(sy + mbC * 128 + 8 + oct, echo, RLX_AGENT);
    refresh(2, true);
    BAR();
    refresh(2, false);
    return;
  }
  if (w >= 8) {
    const unsigned kbase = l4 * 256 + 16 * (l15 ^ l4);
    const unsigned vV = l4 * 256 + 16 * (l15 ^ (l4 << 2));
    const size_t kvoff = ((size_t)b * NH + 2 * hp) * SK * DH;
    const rsrc_t rK = mkrsrc((br ? kB : kA) + kvoff, 2 * SK * DH * 2);
    const rsrc_t rV = mkrsrc((br ? vB : vA) + kvoff, 2 * SK * DH * 2);
    const unsigned rotk = b + hp, half = (w - 8) * 8192;
    unsigned slot = ldsb + half;
    auto issue = [&](unsigned n) {
      const unsigned so = (n >> 4) * 8 * SLOT + (((n >> 1) + rotk) & 7) * SLOT + half;
      if (n & 1) { dma4_kv(rV, vV, vV, vV, vV, so, slot); dma4_kv(rV, vV, vV, vV, vV, so + 4096, slot + 4096); }
      else { dma4_kv(rK, kbase, kbase ^ 64, kbase ^ 128, kbase ^ 192, so, slot); dma4_kv(rK, kbase, kbase ^ 64, kbase ^ 128, kbase ^ 192, so + 4096, slot + 4096); }
      slot += SLOT; if (slot >= ldsb + 6 * SLOT) slot = ldsb + half;
    };
    u32x4 junk = {};
    {
      const char* pc = (const char*)(pc0 & ~(unsigned long long)255) + (w - 8) * 1024;
#pragma nounroll
      for (unsigned k = 0; k < CODE_KB / 2; ++k) asm volatile("global_load_dwordx4 %0, %1, %2" : "+v"(junk) : "v"(lane * 16 + k * 2048), "s"(pc) : "memory");
    }
    BAR();
    BAR(); issue(0); issue(1);
    BAR(); BAR(); BAR(); BAR();
    BAR(); issue(2);
    BAR(); BAR();
    wait_bar<16>(); issue(3);
#pragma nounroll
    for (unsigned n = 1; n < 29; n += 2) { BAR(); issue(n + 3); wait_bar<16>(); issue(n + 4); }
    BAR();
    wait_bar<8>(); BAR();
    wait_bar<0>();
    asm volatile("" :: "v"(junk));
    return;
  }

  const unsigned r32 = lane & 31, hi = lane >> 5, g = w >> 2, wq = w & 3;
  const unsigned tq = l15 >> 2, tp = lane & 3, blk = l4 & 1;
  const unsigned tr0 = 256 * tq + 64 * tq + 32 * blk + 16 * (tp & 1) + 8 * (tp >> 1);
  const unsigned xrow = (32 * wq + r32 < SQ) ? 32 * wq + r32 : 32 * wq + r32 - 16;
  const unsigned xa0 = 16384 + 128 * xrow + 16 * (hi ^ ((xrow >> 1) & 7));
  const unsigned ka0 = 256 * r32 + 16 * (hi ^ (r32 & 15));

  f32x16 acc[4];
#pragma unroll
  for (int c = 0; c < 4; ++c) acc[c] = f32x16{};
  const rsrc_t rW = mkrsrc((br ? wqB : wqA) + (size_t)hp * 256, (DM * HD - hp * 256) * 2);
  auto issueW = [&](unsigned gg, unsigned u, unsigned buf) {
    const unsigned s0 = gg * 256 + (64 * ((u + rotq) & 7) + 16 * wq) * 8192;
    dma4(rW, l4 * 8192 + 16 * (l15 ^ (l4 << 2)), s0, s0 + 31744, s0 + 2 * 31744, s0 + 3 * 31744, buf + wq * 4096);
  };
#define QFIRST(wb, xb) do { xf[0] = ldsfrag(xb); _Pragma("unroll") for (int i = 0; i < 4; ++i) { lo[i] = trread((wb) ^ (64 * i)); hi_[i] = trread(((wb) ^ (64 * i)) + 1024); } } while (0)
#define QGAP(i) do { acc[(i) & 3] = MFMA(cat(lo[i], hi_[i]), xf[(i) >> 2], acc[(i) & 3]); \
    if ((i) + 4 < 16) { if (((i) & 3) == 0) xf[((i) >> 2) + 1] = ldsfrag(xb ^ (32 * (((i) >> 2) + 1))); \
      lo[(i) + 4] = trread((wb ^ (64 * ((i) & 3))) + 4096 * (((i) + 4) >> 2)); hi_[(i) + 4] = trread((wb ^ (64 * ((i) & 3))) + 4096 * (((i) + 4) >> 2) + 1024); } } while (0)
  if (g) {
    issueW(0, 0, ldsb + 2 * SLOT); issueW(0, 1, ldsb + 4 * SLOT);
#pragma nounroll
    for (unsigned i = 2; i < 8; ++i) { wait_bar<4>(); issueW(0, i, ldsb + (2 + 2 * (i & 3)) * SLOT); }
    wait_bar<4>(); wait_bar<0>();
    BAR(); issueW(1, 0, ldsb + LDS_R0);
    BAR(); issueW(1, 1, ldsb + LDS_R1);
#pragma nounroll
    for (unsigned u = 0; u < 8; ++u) {
      const unsigned buf = ldsb + LDS_R0 + (u & 1) * PBUF, wb = buf + hi * 2048 + tr0, xb = buf + xa0;
      bf16x8 xf[4]; s16x4 lo[16], hi_[16];
      if (u < 7) wait_bar<4>(); else wait_bar<0>();
#pragma unroll
      for (int i = 0; i < 16; ++i) {
        if ((i & 3) == 0) xf[i >> 2] = ldsfrag(xb ^ (32 * (i >> 2)));
        lo[i] = trread((wb ^ (64 * (i & 3))) + 4096 * (i >> 2)); hi_[i] = trread((wb ^ (64 * (i & 3))) + 4096 * (i >> 2) + 1024);
      }
      SBAR();
#pragma unroll
      for (int i = 0; i < 8; ++i) { acc[i & 3] = MFMA(cat(lo[i], hi_[i]), xf[i >> 2], acc[i & 3]); SBAR(); }
      if (u < 7) LBAR();
      if (u < 6) issueW(1, u + 2, buf);
      SBAR();
#pragma unroll
      for (int i = 8; i < 16; ++i) { acc[i & 3] = MFMA(cat(lo[i], hi_[i]), xf[i >> 2], acc[i & 3]); SBAR(); }
    }
    BAR();
  } else {
    bf16x8 xf[4]; s16x4 lo[16], hi_[16];
    unsigned wb = ldsb + 2 * SLOT + hi * 2048 + tr0, xb = ldsb + 2 * SLOT + xa0;
    BAR();
    QFIRST(wb, xb);
#pragma nounroll
    for (unsigned u = 0; u < 8; ++u) {
      const unsigned d = (u & 3) == 3 ? -3 * PBUF : PBUF, wn = wb + d, xn = xb + d;
      SBAR();
#pragma unroll
      for (int i = 0; i < 16; ++i) {
        QGAP(i);
        if (i >= 12) { if (i == 12) xf[0] = ldsfrag(xn); lo[i - 12] = trread(wn ^ (64 * (i & 3))); hi_[i - 12] = trread((wn ^ (64 * (i & 3))) + 1024); }
        SBAR();
        if (i == 11) { BAR(); SBAR(); }
      }
      wb = wn; xb = xn;
    }
  }
  bf16x8 qf[8];
#pragma unroll
  for (int c = 0; c < 4; ++c) { acc[c] *= SM_C; qf[2 * c] = pack8(acc[c], 0); qf[2 * c + 1] = pack8(acc[c], 8); }
  if (32 * wq + r32 >= SQ) {
#pragma unroll
    for (int c = 0; c < 8; ++c) qf[c] = bf16x8{};
  }

#pragma unroll
  for (int c = 0; c < 4; ++c) acc[c] = f32x16{};
  float m = 0.f, l = 0.f, lo_thr = 0.f;
  unsigned ksl = ldsb + (g ? 4 * SLOT : 0), vsl = ksl;
  float l2 = 0.f;
  f32x16 s0, s1; unsigned pA[8], pB[8];
#define PFR(P, k) __builtin_bit_cast(bf16x8, u32x4{P[4 * (k)], P[4 * (k) + 1], P[4 * (k) + 2], P[4 * (k) + 3]})
  s16x4 vlo[8], vhi[8]; bf16x8 kf[8];
#define NEXTSLOT(x) ((x) + SLOT == ldsb + 6 * SLOT ? ldsb : (x) + SLOT)
#define VRD(i, vb, vo) do { vlo[i] = trread(((vb) ^ (64 * ((i) & 3))) + (vo) + 4096 * ((i) >> 2)); vhi[i] = trread(((vb) ^ (64 * ((i) & 3))) + (vo) + 4096 * ((i) >> 2) + 2048); } while (0)
#define ELO(i) ((i) < 4 ? 0 : (16 * ((i) - 4)) / 12)
#define HSTEP(DOPV, DOQK, SC, SN, PI, PO, vb, vo, kb, ko, vn, vno) do { \
    float mx0, mx1, alpha = 1.f, pe[16]; bool pend = false; \
    SBAR(); \
    _Pragma("unroll") for (int i = 0; i < 16; ++i) { \
      if (i < 8) { if (DOPV) acc[i & 3] = MFMA(cat(vlo[i], vhi[i]), PFR(PI, i >> 2), acc[i & 3]); } \
      else if (DOQK) { if (i == 8) SN = MFMA(kf[0], qf[0], f32x16{}); else SN = MFMA(kf[i - 8], qf[i - 8], SN); } \
      if (i & 1) { _Pragma("unroll") for (int f = i + 4; f >= i + 3; --f) {     \
          if (f < 8) { if (DOPV) VRD(f, vb, vo); } else if (f < 16) { if (DOQK) kf[f - 8] = ldsfrag(((kb) ^ (32 * (f - 8))) + (ko)); } else VRD(f - 16, vn, vno); } } \
      if (i == 1) { mx0 = max3f(SC[0], SC[1], SC[2]); mx1 = max3f(SC[8], SC[9], SC[10]); mx0 = max3f(mx0, SC[3], SC[4]); mx1 = max3f(mx1, SC[11], SC[12]); PIN(mx0); PIN(mx1); } \
      if (i == 2) { mx0 = max3f(SC[5], SC[6], mx0); mx1 = max3f(SC[13], SC[14], mx1); PIN(mx0); PIN(mx1); mx0 = max3f(SC[7], SC[15], mx0); PIN(mx0); } \
      if (i == 3) {     \
        if (__builtin_expect(__builtin_amdgcn_ballot_w64(!(max3v(mx0, mx1, lo_thr) < SM_HI)) != 0, 0)) { \
          lo_thr = SM_HI; mx0 = __builtin_fmaxf(mx0, mx1); \
          if (__builtin_amdgcn_ballot_w64(!(mx0 - m <= SM_THR)) != 0) {     \
            auto sw = __builtin_amdgcn_permlane32_swap(__float_as_uint(mx0), __float_as_uint(mx0), false, false); \
            const float mn = max3f(m, __uint_as_float(sw[0]), __uint_as_float(sw[1])); alpha = __builtin_amdgcn_exp2f(m - mn); m = mn; l *= alpha; l2 *= alpha; pend = true; } \
          _Pragma("unroll") for (int e = 0; e < 16; ++e) SC[e] -= m; } } \
      if (i >= 5) { _Pragma("unroll") for (int e = ELO(i - 1); e < ELO(i); ++e) { if (e & 1) { l2 += pe[e]; PO[e >> 1] = cvtpk(pe[e - 1], pe[e]); PIN(PO[e >> 1]); } else l += pe[e]; } PIN(l); PIN(l2); } \
      if (i >= 4) { _Pragma("unroll") for (int e = ELO(i); e < ELO(i + 1); ++e) { pe[e] = __builtin_amdgcn_exp2f(SC[e]); PIN(pe[e]); } } \
      SBAR(); \
      if (i == 7) { if (__builtin_expect(pend, 0)) { _Pragma("unroll") for (int c = 0; c < 4; ++c) acc[c] *= alpha; } SBAR(); } \
      if (i == 12) { BAR(); SBAR(); } \
    } \
    _Pragma("unroll") for (int e = ELO(15); e < 16; ++e) { if (e & 1) { l2 += pe[e]; PO[e >> 1] = cvtpk(pe[e - 1], pe[e]); } else l += pe[e]; } \
    PIN(PO[7]); PIN(l); PIN(l2); \
  } while (0)
  if (!g) LBAR();
  {
    const unsigned kb = ksl + ka0;
#pragma unroll
    for (int i = 0; i < 8; ++i) kf[i] = ldsfrag(kb ^ (32 * i));
    s0 = MFMA(kf[0], qf[0], f32x16{});
#pragma unroll
    for (int i = 1; i < 8; ++i) s0 = MFMA(kf[i], qf[i], s0);
    {
      float mx = max3f(s0[0], s0[1], s0[2]);
#pragma unroll
      for (int e = 3; e < 15; e += 2) mx = max3f(mx, s0[e], s0[e + 1]);
      mx = __builtin_fmaxf(mx, s0[15]);
      auto sw = __builtin_amdgcn_permlane32_swap(__float_as_uint(mx), __float_as_uint(mx), false, false);
      mx = __builtin_fmaxf(__uint_as_float(sw[0]), __uint_as_float(sw[1]));
      if (__builtin_amdgcn_ballot_w64(!(mx >= -SM_LO)) != 0) { lo_thr = SM_HI; m = mx; }
    }
    SBAR();
    BAR();
  }
  {
    vsl = NEXTSLOT(ksl);
    const unsigned kb0 = ksl + ka0, vb1 = vsl + hi * 1024 + tr0;
    HSTEP(false, true, s0, s1, pB, pA, vb1, 0, kb0, 8192, vb1, 0);
    ksl = NEXTSLOT(vsl);
  }
#pragma nounroll
  for (unsigned j = 0; j < 7; ++j) {
    const unsigned vnext = NEXTSLOT(ksl);
    const unsigned vb1 = vsl + hi * 1024 + tr0, kb1 = ksl + ka0, vb2 = vnext + hi * 1024 + tr0;
    HSTEP(true, true, s1, s0, pA, pB, vb1, 0, kb1, 0, vb1, 8192);
    unsigned kb2 = kb1; PIN(kb2);
    HSTEP(true, true, s0, s1, pB, pA, vb1, 8192, kb2, 8192, vb2, 0);
    vsl = vnext; ksl = NEXTSLOT(vnext);
  }
  {
    const unsigned vb1 = vsl + hi * 1024 + tr0;
    HSTEP(true, false, s1, s0, pA, pB, vb1, 0, vb1, 0, vb1, 8192);
  }
  {
    const unsigned vb = vsl + hi * 1024 + tr0;
    SBAR();
#pragma unroll
    for (int i = 0; i < 8; ++i) {
      acc[i & 3] = MFMA(cat(vlo[i], vhi[i]), PFR(pB, i >> 2), acc[i & 3]);
      if (i < 4) VRD(i + 4, vb, 8192);
      SBAR();
    }
  }
  unsigned laneT; asm volatile("v_mbcnt_lo_u32_b32 %0, -1, 0\n\tv_mbcnt_hi_u32_b32 %0, -1, %0" : "=v"(laneT));
  const unsigned r32T = laneT & 31, hiT = laneT >> 5, tqT = (laneT & 15) >> 2, tpT = laneT & 3, blkT = (laneT >> 4) & 1;
  const unsigned vo16 = (32 * wq + r32T < SQ) ? laneT * 16 : 0x80000000u;
  const rsrc_t rOf = mkrsrc(ws + WS_O + ((((size_t)br * NB + b) * 4 + wq) * 256 + oct * 64) * 1024, 65536);
  bf16x8 of[8];
  {
    const float inv = 1.f / swap32_add(l + l2);
    __builtin_amdgcn_s_setprio(3);
#pragma unroll
    for (int c = 0; c < 4; ++c) {
      acc[c] *= inv; of[2 * c] = pack8(acc[c], 0); of[2 * c + 1] = pack8(acc[c], 8);
      __builtin_amdgcn_raw_buffer_store_b128(__builtin_bit_cast(u32x4, of[2 * c]), rOf, vo16, (2 * hpl + g) * 8192 + 2 * c * 1024, 16);
      __builtin_amdgcn_raw_buffer_store_b128(__builtin_bit_cast(u32x4, of[2 * c + 1]), rOf, vo16, (2 * hpl + g) * 8192 + (2 * c + 1) * 1024, 16);
      if (c == 1 && !g) BAR();
    }
    __builtin_amdgcn_s_setprio(0);
  }
#pragma unroll
  for (int c = 0; c < 4; ++c) acc[c] = f32x16{};
  const unsigned tro = ldsb + hiT * 2048 + 256 * tqT + 64 * tqT + 32 * (tpT & 1) + 16 * blkT + 8 * (tpT >> 1);
#define NBAR(n) do { _Pragma("nounroll") for (unsigned i_ = (n); i_ > 0; --i_) LBAR(); } while (0)
#define OITEM(F, fo, e) do { const unsigned wb = wo_slot(e) + tro; s16x4 lo[16], hi_[16]; SBAR(); \
    _Pragma("unroll") for (int i = 0; i < 4; ++i) { lo[i] = trread(wb ^ (64 * i)); hi_[i] = trread((wb ^ (64 * i)) + 1024); } \
    SBAR(); \
    _Pragma("unroll") for (int i = 0; i < 16; ++i) {     \
      acc[i & 3] = MFMA(cat(lo[i], hi_[i]), F[(fo) + (i >> 2)], acc[i & 3]); \
      if (i + 4 < 16) { lo[i + 4] = trread((wb ^ (64 * (i & 3))) + 4096 * ((i + 4) >> 2)); hi_[i + 4] = trread((wb ^ (64 * (i & 3))) + 4096 * ((i + 4) >> 2) + 1024); } \
      SBAR(); } } while (0)
  NBAR(g ? 0 : 1); OITEM(of, 0, 8 * g);
  NBAR(g ? 0 : 2); OITEM(of, 4, 8 * g + 1);
  NBAR(g ? 0 : 4);
  wait_bar<0>();
#pragma nounroll
  for (unsigned r = g ? 2 : 0; r < 3; ++r) {
    NBAR(r == 0 ? 7 : r == 1 ? 0 : 1);
    const unsigned e0 = r == 2 ? 10 + 3 * g : 2 + 3 * r;
    bf16x8 pf[12];
#pragma unroll
    for (int k = 0; k < 3; ++k) {
      const unsigned e = e0 + k, t = e >> 1, so = (2 * ((hpl + t) & 3) + (t >> 2)) * 8192 + (e & 1) * 4096;
#pragma unroll
      for (int s = 0; s < 4; ++s) pf[4 * k + s] = __builtin_bit_cast(bf16x8, __builtin_amdgcn_raw_buffer_load_b128(rOf, vo16, so + s * 1024, 16));
    }
    NBAR(r == 1);
    OITEM(pf, 0, e0); OITEM(pf, 4, e0 + 1); OITEM(pf, 8, e0 + 2);
  }
  {
    LAS f32x4* xw = (LAS f32x4*)(size_t)(ldsb + 4 * SLOT + w * 8192 + laneT * 16);
    const LAS f32x4* xr = (const LAS f32x4*)(size_t)(ldsb + 4 * SLOT + (w ^ 4) * 8192 + laneT * 16);
#define XSEND(A, c) _Pragma("unroll") for (int q4 = 0; q4 < 4; ++q4) xw[((c) * 4 + q4) * 64] = f32x4{A[4 * q4], A[4 * q4 + 1], A[4 * q4 + 2], A[4 * q4 + 3]}
    if (g) { XSEND(acc[0], 0); XSEND(acc[1], 1); acc[0] = acc[2]; acc[1] = acc[3]; } else { XSEND(acc[2], 0); XSEND(acc[3], 1); }
    LBAR();
#pragma unroll
    for (int c = 0; c < 2; ++c)
#pragma unroll
      for (int q4 = 0; q4 < 4; ++q4) { const f32x4 o = xr[(c * 4 + q4) * 64];
#pragma unroll
        for (int k = 0; k < 4; ++k) acc[c][4 * q4 + k] += o[k]; }
  }
  const rsrc_t rP = mkrsrc(ws + WS_P + (((size_t)br * NB + b) * 4 + hpl) * 262144, 262144);
#pragma unroll
  for (int c = 0; c < 2; ++c) {
    if (2 * g + c != oct) {
      __builtin_amdgcn_raw_buffer_store_b128(__builtin_bit_cast(u32x4, pack8(acc[c], 0)), rP, vo16, (((2 * g + c) * 4 + oct) * 4 + wq) * 4096, 16);
      __builtin_amdgcn_raw_buffer_store_b128(__builtin_bit_cast(u32x4, pack8(acc[c], 8)), rP, vo16, (((2 * g + c) * 4 + oct) * 4 + wq) * 4096 + 1024, 16);
    }
  }
  if (oct & 1) acc[0] = acc[1];
  VM0();
  BAR();
  if (g != (oct >> 1)) return;
  BAR();
  {
    u32x4 pv[6];
#pragma unroll
    for (int k = 0; k < 3; ++k)
#pragma unroll
      for (int h2 = 0; h2 < 2; ++h2)
        pv[2 * k + h2] = __builtin_amdgcn_raw_buffer_load_b128(rP, vo16, ((oct * 4 + ((oct + 1 + k) & 3)) * 4 + wq) * 4096 + h2 * 1024, 16);
#pragma unroll
    for (int k = 0; k < 3; ++k)
#pragma unroll
      for (int r = 0; r < 16; ++r) { const unsigned u = pv[2 * k + (r >> 3)][(r >> 1) & 3]; acc[0][r] += __uint_as_float((r & 1) ? (u & 0xffff0000u) : (u << 16)); }
    const unsigned q = 32 * wq + r32T;
    if (q < SQ) {
      u16* o = out + ((size_t)b * SQ + q) * 1024 + br * 512 + hpl * 128 + oct * 32 + hiT * 16;
      *(bf16x8*)o = pack8(acc[0], 0);
      *(bf16x8*)(o + 8) = pack8(acc[0], 8);
    }
  }
}

extern "C" void kernel_launch(void* const* d_in, const int* in_sizes, int n_in, void* d_out, int out_size, void* d_ws, size_t ws_size, hipStream_t stream) {
  const u16* xA = (const u16*)d_in[0]; const u16* kA = (const u16*)d_in[1]; const u16* vA = (const u16*)d_in[2]; const u16* wqA = (const u16*)d_in[3]; const u16* woA = (const u16*)d_in[4];
  const u16* xB = (const u16*)d_in[5]; const u16* kB = (const u16*)d_in[6]; const u16* vB = (const u16*)d_in[7]; const u16* wqB = (const u16*)d_in[8]; const u16* woB = (const u16*)d_in[9];
  (void)hipFuncSetAttribute((const void*)fused_kernel, hipFuncAttributeMaxDynamicSharedMemorySize, LDS_BYTES);
  fused_kernel<<<256, NTHREADS, LDS_BYTES, stream>>>(xA, kA, vA, wqA, woA, xB, kB, vB, wqB, woB, (char*)d_ws, (u16*)d_out);
}
```
